```python
import torch

def _softmax(x, axis=-1):
    return torch.softmax(x, dim=axis)
def _gelu(x):
    return torch.nn.functional.gelu(x, approximate="tanh")
def _silu(x):
    return torch.nn.functional.silu(x)
def _sigmoid(x):
    return torch.sigmoid(x)
def _layernorm(x, axis=-1, eps=1e-5):
    m = x.mean(dim=axis, keepdim=True)
    v = x.var(dim=axis, keepdim=True, unbiased=False)
    return (x - m) / torch.sqrt(v + eps)
def _scatter_add(tgt, idx, src, axis=0):
    return tgt.clone().index_add_(axis, idx, src)
def _segment_sum(src, seg, n):
    out = torch.zeros((n,) + src.shape[1:], dtype=src.dtype)
    return out.index_add_(0, seg, src)
def _pad(x, pw):
    flat = tuple(v for pair in reversed(pw) for v in pair)
    return torch.nn.functional.pad(x, flat)

kernel_name = 'gated_mix__sliding_local_dilated__softmax_full_mha__prompt'

def setup_inputs():

    _w = (lambda a: (lambda t: t.to(torch.bfloat16) if t.is_floating_point() else t)(torch.as_tensor(a)))
    def _A_setup():
        B, S, D = 1, 2048, 512
        return {
            "x": _w((lambda *s: torch.randn(*s, dtype=torch.bfloat16) * 0.05)(B, S, D)),
            "wqkv": _w((lambda *s: torch.randn(*s, dtype=torch.bfloat16) * 1.6)(D, 3 * D)),
            "wo": _w((lambda *s: torch.randn(*s, dtype=torch.bfloat16) * 0.05)(D, D)),
        }
    _A_in = _A_setup()

    def _B_setup():
        B, S, D = 1, 2048, 512
        return {
            "x": _w((lambda *s: torch.randn(*s, dtype=torch.bfloat16) * 0.05)(B, S, D)),
            "wqkv": _w((lambda *s: torch.randn(*s, dtype=torch.bfloat16) * 1.6)(D, 3 * D)),
            "wo": _w((lambda *s: torch.randn(*s, dtype=torch.bfloat16) * 0.1)(D, D)),
        }
    _B_in = _B_setup()

    return {
        "A_x": _A_in["x"],
        "A_wqkv": _A_in["wqkv"],
        "A_wo": _A_in["wo"],
        "B_x": _B_in["x"],
        "B_wqkv": _B_in["wqkv"],
        "B_wo": _B_in["wo"],
        "gate_w": _w((lambda *s: torch.randn(*s, dtype=torch.bfloat16) * 0.05)(512) * 16.0),
    }

def reference(A_x, A_wqkv, A_wo, B_x, B_wqkv, B_wo, gate_w):
    def _A_ref(x, wqkv, wo):
        B, S, D = x.shape
        H = 32; Dh = D // H
        W, dil = 8, 2
        qkv = (lambda x, *d: x.permute(*d))((x @ wqkv).reshape(B, S, 3, H, Dh), 2, 0, 3, 1, 4)
        q, k, v = qkv[0], qkv[1], qkv[2]
        scale = Dh ** -0.5
        pos = torch.arange(S)
        off = torch.arange(W) * dil
        idx = pos[:, None] - off[None, :]
        mask = idx >= 0
        idx_c = torch.where(mask, idx, 0).reshape(-1)
        kg = k[:, :, idx_c, :].reshape(B, H, S, W, Dh)
        vg = v[:, :, idx_c, :].reshape(B, H, S, W, Dh)
        logits = torch.einsum("bhsd,bhswd->bhsw", q, kg) * scale
        logits = torch.where(mask[None, None, :, :], logits, -1e9)
        attn = _softmax(logits, axis=-1)
        o = torch.einsum("bhsw,bhswd->bhsd", attn, vg)
        return (lambda x, *d: x.permute(*d))(o, 0, 2, 1, 3).reshape(B, S, D) @ wo

    def _B_ref(x, wqkv, wo):
        B, S, D = x.shape
        H = 32; Dh = D // H
        qkv = (lambda x, *d: x.permute(*d))((x @ wqkv).reshape(B, S, 3, H, Dh), 2, 0, 3, 1, 4)
        q, k, v = qkv[0], qkv[1], qkv[2]
        scale = Dh ** -0.5
        scores = torch.einsum("bhqd,bhkd->bhqk", q, k) * scale
        p = _softmax(scores, axis=-1)
        o = torch.einsum("bhqk,bhkd->bhqd", p, v)
        return (lambda x, *d: x.permute(*d))(o, 0, 2, 1, 3).reshape(B, S, D) @ wo

    x = A_x
    g = _sigmoid((x * gate_w).sum(-1, keepdims=True))
    a = _A_ref(x, A_wqkv, A_wo)
    b = _B_ref(x, B_wqkv, B_wo)
    return g * a + (1 - g) * b
```

```cpp
#include <hip/hip_runtime.h>
#include <hip/hip_bf16.h>
typedef __hip_bfloat16 bf16;
constexpr int S = 2048, D = 512, H = 32, DH = 16;
__device__ inline float b2f(bf16 x) { return __bfloat162float(x); }
__global__ void gemm_naive(const bf16* A, const bf16* B, bf16* C, int M, int N, int K) {
    int n = blockIdx.x * 64 + threadIdx.x, m = blockIdx.y;
    float acc = 0;
    for (int k = 0; k < K; ++k) acc += b2f(A[m * K + k]) * b2f(B[k * N + n]);
    C[m * N + n] = __float2bfloat16(acc);
}
__global__ void attn_full(const bf16* qkv, bf16* o) {
    int s = blockIdx.x * 64 + threadIdx.x, h = blockIdx.y;
    float q[DH], acc[DH]; for (int d = 0; d < DH; ++d) { q[d] = b2f(qkv[s * 1536 + h * 16 + d]) * 0.25f; acc[d] = 0; }
    float m = -1e30f, l = 0;
    for (int k = 0; k < S; ++k) {
        float sc = 0; for (int d = 0; d < DH; ++d) sc += q[d] * b2f(qkv[k * 1536 + 512 + h * 16 + d]);
        float mn = fmaxf(m, sc), a = __expf(m - mn), p = __expf(sc - mn); l = l * a + p;
        for (int d = 0; d < DH; ++d) acc[d] = acc[d] * a + p * b2f(qkv[k * 1536 + 1024 + h * 16 + d]);
        m = mn;
    }
    for (int d = 0; d < DH; ++d) o[s * 512 + h * 16 + d] = __float2bfloat16(acc[d] / l);
}
__global__ void attn_local(const bf16* qkv, bf16* o) {
    int s = blockIdx.x * 64 + threadIdx.x, h = blockIdx.y;
    float q[DH], acc[DH]; for (int d = 0; d < DH; ++d) { q[d] = b2f(qkv[s * 1536 + h * 16 + d]) * 0.25f; acc[d] = 0; }
    float m = -1e30f, l = 0;
    for (int w = 0; w < 8; ++w) { int k = s - 2 * w; if (k < 0) break;
        float sc = 0; for (int d = 0; d < DH; ++d) sc += q[d] * b2f(qkv[k * 1536 + 512 + h * 16 + d]);
        float mn = fmaxf(m, sc), a = __expf(m - mn), p = __expf(sc - mn); l = l * a + p;
        for (int d = 0; d < DH; ++d) acc[d] = acc[d] * a + p * b2f(qkv[k * 1536 + 1024 + h * 16 + d]);
        m = mn;
    }
    for (int d = 0; d < DH; ++d) o[s * 512 + h * 16 + d] = __float2bfloat16(acc[d] / l);
}
__global__ void outproj(const bf16* x, const bf16* gw, const bf16* oa, const bf16* ob, const bf16* wa, const bf16* wb, bf16* out) {
    int n = blockIdx.x * 64 + threadIdx.x, m = blockIdx.y;
    float g = 0; for (int k = 0; k < D; ++k) g += b2f(x[m * D + k]) * b2f(gw[k]);
    g = 1.f / (1.f + __expf(-g));
    float a = 0, b = 0;
    for (int k = 0; k < D; ++k) { a += b2f(oa[m * D + k]) * b2f(wa[k * D + n]); b += b2f(ob[m * D + k]) * b2f(wb[k * D + n]); }
    out[m * D + n] = __float2bfloat16(g * a + (1.f - g) * b);
}
extern "C" void kernel_launch(void* const* d_in, const int* in_sizes, int n_in, void* d_out, int out_size, void* d_ws, size_t ws_size, hipStream_t stream) {
    const bf16* x = (const bf16*)d_in[0]; const bf16* wqa = (const bf16*)d_in[1]; const bf16* woa = (const bf16*)d_in[2];
    const bf16* wqb = (const bf16*)d_in[4]; const bf16* wob = (const bf16*)d_in[5]; const bf16* gw = (const bf16*)d_in[6];
    bf16* qa = (bf16*)d_ws; bf16* qb = qa + S * 1536; bf16* oa = qb + S * 1536; bf16* ob = oa + S * D;
    gemm_naive<<<dim3(24, S), 64, 0, stream>>>(x, wqa, qa, S, 1536, D);
    gemm_naive<<<dim3(24, S), 64, 0, stream>>>(x, wqb, qb, S, 1536, D);
    attn_local<<<dim3(S / 64, H), 64, 0, stream>>>(qa, oa);
    attn_full<<<dim3(S / 64, H), 64, 0, stream>>>(qb, ob);
    outproj<<<dim3(8, S), 64, 0, stream>>>(x, gw, oa, ob, woa, wob, (bf16*)d_out);
}
```

```cpp
#include <hip/hip_runtime.h>
#include <hip/hip_bf16.h>
#include <stdint.h>

#define STAMPS 0

#define GAS __attribute__((address_space(1)))
#define LAS __attribute__((address_space(3)))
#define RLX_AGENT __ATOMIC_RELAXED, __HIP_MEMORY_SCOPE_AGENT
typedef unsigned u32x2 __attribute__((ext_vector_type(2)));
typedef unsigned u32x4 __attribute__((ext_vector_type(4)));
typedef unsigned u32x8 __attribute__((ext_vector_type(8)));
typedef float f32x4 __attribute__((ext_vector_type(4)));
typedef float f32x8 __attribute__((ext_vector_type(8)));
typedef float f32x16 __attribute__((ext_vector_type(16)));
typedef __bf16 bf16x8 __attribute__((ext_vector_type(8)));
typedef __bf16 bf16x16 __attribute__((ext_vector_type(16)));
typedef short s16x4 __attribute__((ext_vector_type(4)));
typedef GAS unsigned long long gu64;
typedef LAS const char* lds_cptr;

namespace {
constexpr int SEQ = 2048, DM = 512;
constexpr unsigned WS_EPOCH = 0;
constexpr unsigned WS_KVFLAG = 4096;
constexpr unsigned WS_OAFLAG = 8192;
constexpr unsigned WS_OBFLAG = 16384;
constexpr unsigned WS_STAMP = 32768;
constexpr unsigned WS_QKV = 1u << 20;
constexpr unsigned WS_OA = WS_QKV + 6u * (1u << 20);
constexpr unsigned WS_OB = WS_OA + 2u * (1u << 20);
constexpr unsigned WS_END = WS_OB + 2u * (1u << 20);
constexpr int XSTR = 1056;
constexpr int X_OFF = 0, WR_OFF = 80 * XSTR, WSLOT = 24576;
constexpr int GATE_OFF = 159488, ONES_OFF = 159744, LDS_BYTES = 163840;
constexpr int K_OFF = 0, V_OFF = 65536;
constexpr int WOA_OFF = 0, WOB_OFF = 65536, RED_OFF = 131072;
static_assert(WR_OFF + 3 * WSLOT <= GATE_OFF, "lds");
constexpr float QSCALE = 0.25f * 1.4426950408889634f;
constexpr unsigned kSpinCap = 1u << 18;

#define SBAR() __builtin_amdgcn_sched_barrier(0)
#define PIN(x) asm volatile("" : "+v"(x))

typedef float f32x2 __attribute__((ext_vector_type(2)));
typedef __bf16 bf16x2 __attribute__((ext_vector_type(2)));
__device__ __forceinline__ unsigned cvtpk(float lo, float hi) { return __builtin_bit_cast(unsigned, __builtin_convertvector((f32x2){lo, hi}, bf16x2)); }
__device__ __forceinline__ void glds1(const void* sbase, unsigned voff, unsigned ldsb) {
    unsigned sv; asm volatile("s_mov_b32 %0, m0\n\ts_mov_b32 m0, %3\n\ts_nop 0\n\tglobal_load_lds_dwordx4 %1, %2\n\ts_mov_b32 m0, %0" : "=&s"(sv) : "v"(voff), "s"(sbase), "s"(ldsb) : "memory"); }
__device__ __forceinline__ void glds3(const void* sbase, unsigned voff, unsigned ldsb) {
    unsigned sv; asm volatile("s_mov_b32 %0, m0\n\ts_mov_b32 m0, %3\n\ts_nop 0\n\tglobal_load_lds_dwordx4 %1, %2\n\tglobal_load_lds_dwordx4 %1, %2 offset:1024\n\tglobal_load_lds_dwordx4 %1, %2 offset:2048\n\ts_mov_b32 m0, %0"
                              : "=&s"(sv) : "v"(voff), "s"(sbase), "s"(ldsb) : "memory"); }
__device__ __forceinline__ u32x2 tr64(lds_cptr p) { return __builtin_bit_cast(u32x2, __builtin_amdgcn_ds_read_tr16_b64_v4i16((LAS s16x4*)p)); }
__device__ __forceinline__ u32x4 lds128(lds_cptr p) { return *(LAS const u32x4*)p; }
__device__ __forceinline__ f32x4 mfma16(u32x4 a, u32x4 b, f32x4 c) { return __builtin_amdgcn_mfma_f32_16x16x32_bf16(__builtin_bit_cast(bf16x8, a), __builtin_bit_cast(bf16x8, b), c, 0, 0, 0); }
__device__ __forceinline__ f32x4 mfma16k16(u32x2 a, u32x2 b, f32x4 c) { return __builtin_amdgcn_mfma_f32_16x16x16bf16_1k(__builtin_bit_cast(s16x4, a), __builtin_bit_cast(s16x4, b), c, 0, 0, 0); }
__device__ __forceinline__ f32x16 mfma32(u32x4 a, u32x4 b, f32x16 c) { return __builtin_amdgcn_mfma_f32_32x32x16_bf16(__builtin_bit_cast(bf16x8, a), __builtin_bit_cast(bf16x8, b), c, 0, 0, 0); }
__device__ __forceinline__ f32x16 smf(u32x4 a, f32x8 b, f32x16 c, int idx) { return __builtin_amdgcn_smfmac_f32_32x32x32_bf16(__builtin_bit_cast(bf16x8, a), __builtin_bit_cast(bf16x16, b), c, idx, 0, 0); }
__device__ __forceinline__ u32x2 pack4(f32x4 v) { return (u32x2){cvtpk(v[0], v[1]), cvtpk(v[2], v[3])}; }
__device__ __forceinline__ u32x4 join16(u32x2 x, u32x2 y) {
    auto a = __builtin_amdgcn_permlane16_swap(x[0], y[0], false, false); auto b = __builtin_amdgcn_permlane16_swap(x[1], y[1], false, false);
    return (u32x4){a[0], b[0], a[1], b[1]}; }
__device__ __forceinline__ float xmax16(float v) { auto r = __builtin_amdgcn_permlane16_swap(__float_as_uint(v), __float_as_uint(v), false, false); return fmaxf(__uint_as_float(r[0]), __uint_as_float(r[1])); }
__device__ __forceinline__ float xmax32(float v) { auto r = __builtin_amdgcn_permlane32_swap(__float_as_uint(v), __float_as_uint(v), false, false); return fmaxf(__uint_as_float(r[0]), __uint_as_float(r[1])); }
__device__ __forceinline__ float xadd16(float v) { auto r = __builtin_amdgcn_permlane16_swap(__float_as_uint(v), __float_as_uint(v), false, false); return __uint_as_float(r[0]) + __uint_as_float(r[1]); }
__device__ __forceinline__ float xadd32(float v) { auto r = __builtin_amdgcn_permlane32_swap(__float_as_uint(v), __float_as_uint(v), false, false); return __uint_as_float(r[0]) + __uint_as_float(r[1]); }
__device__ __forceinline__ f32x8 lo8(const f32x16& v) { return __builtin_shufflevector(v, v, 0, 1, 2, 3, 4, 5, 6, 7); }
__device__ __forceinline__ f32x8 hi8(const f32x16& v) { return __builtin_shufflevector(v, v, 8, 9, 10, 11, 12, 13, 14, 15); }
#define WAIT_BAR(N) asm volatile("s_waitcnt vmcnt(" #N ") lgkmcnt(0)\n\ts_barrier" ::: "memory")

__device__ __forceinline__ void wait_flags(gu64* f, int n, int lane, unsigned long long tag) {
    gu64* p = f + (lane < n ? lane : 0);
    for (unsigned spins = 0; spins < kSpinCap; ++spins) {
        const unsigned long long v = __hip_atomic_load(p, RLX_AGENT);
        if (__all(v == tag)) break;
        __builtin_amdgcn_s_sleep(1);
    }
    __builtin_amdgcn_fence(__ATOMIC_ACQUIRE, "wavefront");
}

#define STAMP(k) do {} while (0)

__global__ __launch_bounds__(512, 2) void fused_kernel(const char* __restrict__ x, const char* __restrict__ wqa, const char* __restrict__ woa,
                                                       const char* __restrict__ wqb, const char* __restrict__ wob, const char* __restrict__ gw,
                                                       char* __restrict__ out, char* ws) {
    __shared__ __attribute__((aligned(256))) char lds[LDS_BYTES];
    const int tid = threadIdx.x, lane = tid & 63, w = __builtin_amdgcn_readfirstlane(tid >> 6);
    const int xcd = blockIdx.x & 7, idx = blockIdx.x >> 3;
    const int g = 2 * (xcd >> 1) + (idx & 1), i = 16 * (xcd & 1) + (idx >> 1);
    const unsigned lds0 = (unsigned)(uintptr_t)(LAS char*)lds;
    const lds_cptr L = (lds_cptr)lds;
    const __amdgpu_buffer_rsrc_t wsr = __builtin_amdgcn_make_buffer_rsrc(ws, 0, WS_END, 0x00020000);
    STAMP(0);
    const int rot = (idx >> 1) & 7;
    {
#pragma unroll
        for (int r = 0; r < 8; ++r) glds1(x + (size_t)(64 * i + 8 * w + r) * 1024, lane * 16, lds0 + X_OFF + (16 + 8 * w + r) * XSTR);
#pragma unroll
        for (int r = 0; r < 2; ++r) { int t = 64 * i - 16 + 2 * w + r; t = t < 0 ? 0 : t; glds1(x + (size_t)t * 1024, lane * 16, lds0 + X_OFF + (2 * w + r) * XSTR); }
    }
    const unsigned wvoff = (8 * w + (lane >> 3)) * 3072 + g * 128 + (((lane & 7) ^ (((((lane >> 4) & 1) | ((w & 1) << 1))) << 1)) * 16);
    const unsigned wdst = lds0 + WR_OFF + w * 3072;
#define WSRC(s) (((s) < 8 ? wqb : wqa) + (size_t)((((s) + rot) & 7) * 64 * 3072))
    glds3(WSRC(0), wvoff, wdst); glds3(WSRC(1), wvoff, wdst + WSLOT);
    unsigned long long tag;
    { unsigned long long z = __hip_atomic_load((gu64*)(ws + WS_EPOCH), RLX_AGENT); const unsigned long long e = z;
      z += 0x9E3779B97F4A7C15ull; z = (z ^ (z >> 30)) * 0xBF58476D1CE4E5B9ull; z = (z ^ (z >> 27)) * 0x94D049BB133111EBull; z ^= z >> 31; tag = z == e ? ~z : z; }
    const u32x4 gwv = *(const u32x4*)(gw + lane * 16);
    if (tid < 256) *(LAS u32x4*)(lds + ONES_OFF + tid * 16) = (u32x4){0x3F803F80u, 0x3F803F80u, 0x3F803F80u, 0x3F803F80u};

    const int hl = w >> 1, tt = w & 1, l15 = lane & 15, lg = lane >> 4;
    const lds_cptr xrd = L + X_OFF + (32 * tt + l15) * XSTR + lg * 16;
    const lds_cptr wrd = L + WR_OFF + lg * 3072 + ((lane >> 2) & 3) * 128 + ((hl ^ (((lane >> 3) & 1) | ((lg & 1) << 1))) * 32) + (lane & 3) * 8;
    int slot = 0;
    {
        f32x4 q0 = {}, q1 = {}, k0 = {}, k1 = {}, v0 = {}, v1 = {};
        for (int s = 0; s < 8; ++s) {
            WAIT_BAR(3);
            { const int ns = slot == 0 ? 2 : slot - 1; glds3(WSRC(s + 2), wvoff, wdst + ns * WSLOT); }
            const lds_cptr xp = xrd + ((s + rot) & 7) * 128, wp = wrd + slot * WSLOT;
#pragma unroll
            for (int cc = 0; cc < 2; ++cc) {
                const u32x4 xa = lds128(xp + 16 * XSTR + cc * 64), xb = lds128(xp + 32 * XSTR + cc * 64);
                u32x4 wf[3];
#pragma unroll
                for (int sec = 0; sec < 3; ++sec) { const u32x2 a = tr64(wp + cc * 12288 + sec * 1024), b = tr64(wp + cc * 12288 + sec * 1024 + 512); wf[sec] = (u32x4){a[0], a[1], b[0], b[1]}; }
                q0 = mfma16(wf[0], xa, q0); q1 = mfma16(wf[0], xb, q1);
                k0 = mfma16(wf[1], xa, k0); k1 = mfma16(wf[1], xb, k1);
                v0 = mfma16(xa, wf[2], v0); v1 = mfma16(xb, wf[2], v1);
            }
            if (s == 0) {
                float gs[8];
#pragma unroll
                for (int r = 0; r < 8; ++r) { const u32x4 xv = lds128(L + X_OFF + (16 + 8 * w + r) * XSTR + lane * 16); float a = 0.f;
#pragma unroll
                    for (int e = 0; e < 4; ++e) { a = fmaf(__uint_as_float(xv[e] << 16), __uint_as_float(gwv[e] << 16), a); a = fmaf(__uint_as_float(xv[e] & 0xffff0000u), __uint_as_float(gwv[e] & 0xffff0000u), a); }
                    gs[r] = a; }
#pragma unroll
                for (int r = 0; r < 8; ++r) {
#pragma unroll
                    for (int m = 1; m < 16; m <<= 1) gs[r] += __shfl_xor(gs[r], m);
                    gs[r] = xadd32(xadd16(gs[r])); }
                float mine = gs[0];
#pragma unroll
                for (int r = 1; r < 8; ++r) mine = lane == r ? gs[r] : mine;
                if (lane < 8) *(LAS float*)(lds + GATE_OFF + (8 * w + lane) * 4) = 1.f / (1.f + __expf(-mine));
            }
            slot = slot == 2 ? 0 : slot + 1;
        }
        q0 *= QSCALE; q1 *= QSCALE;
        const unsigned dst = WS_QKV + (4 * g + hl) * 196608 + (2 * i + tt) * 1024 + lane * 16;
        __builtin_amdgcn_raw_buffer_store_b128(join16(pack4(k0), pack4(k1)), wsr, dst + 65536, 0, 16);
        __builtin_amdgcn_raw_buffer_store_b128(join16(pack4(v0), pack4(v1)), wsr, dst + 131072, 0, 16);
        __builtin_amdgcn_raw_buffer_store_b128(join16(pack4(q0), pack4(q1)), wsr, dst, 0, 16);
    }
    STAMP(1);
    {
        f32x4 q0 = {}, q1 = {}, kp = {}, k0 = {}, k1 = {}, vp = {}, v0 = {}, v1 = {};
        for (int s = 8; s < 16; ++s) {
            if (s == 9 || s == 15) { WAIT_BAR(0); } else if (s == 8) { WAIT_BAR(6); } else { WAIT_BAR(3); }
            if (s == 9 && tid == 0) __hip_atomic_store((gu64*)(ws + WS_KVFLAG) + g * 32 + i, tag, RLX_AGENT);
            if (s < 14) { const int ns = slot == 0 ? 2 : slot - 1; glds3(WSRC(s + 2), wvoff, wdst + ns * WSLOT); }
            const lds_cptr xp = xrd + ((s + rot) & 7) * 128, wp = wrd + slot * WSLOT;
#pragma unroll
            for (int cc = 0; cc < 2; ++cc) {
                const u32x4 xz = lds128(xp + cc * 64), xa = lds128(xp + 16 * XSTR + cc * 64), xb = lds128(xp + 32 * XSTR + cc * 64);
                u32x4 wf[3];
#pragma unroll
                for (int sec = 0; sec < 3; ++sec) { const u32x2 a = tr64(wp + cc * 12288 + sec * 1024), b = tr64(wp + cc * 12288 + sec * 1024 + 512); wf[sec] = (u32x4){a[0], a[1], b[0], b[1]}; }
                q0 = mfma16(wf[0], xa, q0); q1 = mfma16(wf[0], xb, q1);
                kp = mfma16(wf[1], xz, kp); k0 = mfma16(wf[1], xa, k0); k1 = mfma16(wf[1], xb, k1);
                vp = mfma16(xz, wf[2], vp); v0 = mfma16(xa, wf[2], v0); v1 = mfma16(xb, wf[2], v1);
            }
            slot = slot == 2 ? 0 : slot + 1;
        }
        STAMP(2);
        q0 *= QSCALE; q1 *= QSCALE;
        const u32x2 qf[2] = {pack4(q0), pack4(q1)}, kf[3] = {pack4(kp), pack4(k0), pack4(k1)}, vf[3] = {pack4(vp), pack4(v0), pack4(v1)};
        u32x2 of[2];
#pragma unroll
        for (int sb = 0; sb < 2; ++sb) {
            const f32x4 z = {};
            f32x4 sp = mfma16k16(kf[sb], qf[sb], z), sc = mfma16k16(kf[sb + 1], qf[sb], z);
            const bool first = (i == 0 && tt == 0 && sb == 0);
            float m = -3e38f;
#pragma unroll
            for (int r = 0; r < 4; ++r) { const int dc = l15 - (4 * lg + r), dp = dc + 16;
                sc[r] = (dc >= 0 && !(dc & 1)) ? sc[r] : -3e38f; sp[r] = (dp <= 14 && !(dp & 1) && !first) ? sp[r] : -3e38f;
                m = fmaxf(m, fmaxf(sc[r], sp[r])); }
            m = xmax32(xmax16(m));
            float l = 0.f;
#pragma unroll
            for (int r = 0; r < 4; ++r) { sc[r] = __builtin_amdgcn_exp2f(sc[r] - m); sp[r] = __builtin_amdgcn_exp2f(sp[r] - m); l += sc[r] + sp[r]; }
            l = xadd32(xadd16(l));
            f32x4 o = mfma16k16(vf[sb], pack4(sp), z); o = mfma16k16(vf[sb + 1], pack4(sc), o);
            o *= __builtin_amdgcn_rcpf(l);
            of[sb] = pack4(o);
        }
        __builtin_amdgcn_raw_buffer_store_b128(join16(of[0], of[1]), wsr, WS_OA + ((2 * i + tt) * 32 + 4 * g + hl) * 1024 + lane * 16, 0, 16);
    }
    STAMP(3);
    const int h = 4 * g + (i & 3), j = i >> 2;
    u32x4 qf;
    {
        wait_flags((gu64*)(ws + WS_KVFLAG) + g * 32, 32, lane, tag);
        STAMP(4);
        const unsigned src = WS_QKV + h * 196608 + lane * 16;
        u32x4 kv[16];
#pragma unroll
        for (int t = 0; t < 8; ++t) { kv[t] = __builtin_amdgcn_raw_buffer_load_b128(wsr, src + 65536 + (8 * w + t) * 1024, 0, 16); kv[8 + t] = __builtin_amdgcn_raw_buffer_load_b128(wsr, src + 131072 + (8 * w + t) * 1024, 0, 16); }
        qf = __builtin_amdgcn_raw_buffer_load_b128(wsr, src + (8 * j + w) * 1024, 0, 16);
        WAIT_BAR(0);
        if (tid == 0) __hip_atomic_store((gu64*)(ws + WS_OAFLAG) + i * 32 + g, tag, RLX_AGENT);
#pragma unroll
        for (int t = 0; t < 8; ++t) { *(LAS u32x4*)(lds + K_OFF + (8 * w + t) * 1024 + lane * 16) = kv[t]; *(LAS u32x4*)(lds + V_OFF + (8 * w + t) * 1024 + lane * 16) = kv[8 + t]; }
        asm volatile("s_waitcnt lgkmcnt(0)\n\ts_barrier" ::: "memory");
    }
    STAMP(5);
    f32x16 o;
    {
        const bool real = (lane & 16) == 0;
        const lds_cptr kb0 = L + K_OFF + lane * 16;
        const lds_cptr vb0 = L + (real ? V_OFF : ONES_OFF) + (lane >> 5) * 512 + l15 * 16;
        const int vstep = real ? 4096 : 0;
        const int sidx = (int)0xDDDDDDDDu;
        const f32x16 zero = {};
        float m;
        { f32x16 s = mfma32(lds128(kb0), qf, zero); m = s[0];
#pragma unroll
          for (int r = 1; r < 16; ++r) m = fmaxf(m, s[r]);
          m = xmax32(m); }
        for (int attempt = 0;; ++attempt) {
            f32x16 cin;
#pragma unroll
            for (int r = 0; r < 16; ++r) cin[r] = -m;
            PIN(cin);
            o = zero;
            lds_cptr kb = kb0, vb = vb0;
            for (int t4 = 0; t4 < 16; ++t4) {
#pragma unroll
                for (int u = 0; u < 4; ++u) {
                    f32x16 s = mfma32(lds128(kb + u * 1024), qf, cin);
                    const u32x4 va = lds128(vb + u * 1024), vc = lds128(vb + u * 1024 + 256);
#pragma unroll
                    for (int r = 0; r < 16; ++r) s[r] = __builtin_amdgcn_exp2f(s[r]);
                    o = smf(va, lo8(s), o, sidx); o = smf(vc, hi8(s), o, sidx);
                }
                kb += 4096; vb += vstep;
            }
            float chk = o[8];
#pragma unroll
            for (int r = 0; r < 8; ++r) chk = fmaxf(chk, fabsf(o[r]) * 0x1p-30f);
            const bool bad = !(o[8] < 0x1p90f && chk < 0x1p90f);
            if (attempt || !__any(bad)) break;
            m = -3e38f;
            for (int t = 0; t < 64; ++t) { f32x16 s = mfma32(lds128(kb0 + t * 1024), qf, zero);
#pragma unroll
                for (int r = 0; r < 16; ++r) m = fmaxf(m, s[r]); }
            m = xmax32(m);
        }
    }
    STAMP(6);
    {
        const float rl = __builtin_amdgcn_rcpf(o[8]);
        const unsigned a0 = cvtpk(o[0] * rl, o[1] * rl), a1 = cvtpk(o[2] * rl, o[3] * rl), b0 = cvtpk(o[4] * rl, o[5] * rl), b1 = cvtpk(o[6] * rl, o[7] * rl);
        auto p0 = __builtin_amdgcn_permlane32_swap(a0, b0, false, false); auto p1 = __builtin_amdgcn_permlane32_swap(a1, b1, false, false);
        __builtin_amdgcn_raw_buffer_store_b128((u32x4){p0[0], p1[0], p0[1], p1[1]}, wsr, WS_OB + ((8 * j + w) * 32 + h) * 1024 + lane * 16, 0, 16);
        WAIT_BAR(0);
        if (tid == 0) __hip_atomic_store((gu64*)(ws + WS_OBFLAG) + j * 32 + h, tag, RLX_AGENT);
    }
    STAMP(7);
    {
        const unsigned ovoff = (lane >> 3) * 1024 + g * 128 + (((lane & 7) ^ (((lane >> 4) & 1) << 2)) * 16);
#pragma unroll
        for (int u = 0; u < 8; ++u) glds1(woa + (size_t)(64 * w + 8 * u) * 1024, ovoff, lds0 + WOA_OFF + (8 * w + u) * 1024);
#pragma unroll
        for (int u = 0; u < 8; ++u) glds1(wob + (size_t)(64 * w + 8 * u) * 1024, ovoff, lds0 + WOB_OFF + (8 * w + u) * 1024);
        const int ct = (w >> 1) & 1, kh = w >> 2;
        wait_flags((gu64*)(ws + WS_OAFLAG) + i * 32, 8, lane, tag);
        u32x4 of[16];
        const unsigned osrc = ((2 * i + tt) * 32 + 16 * kh) * 1024 + lane * 16;
#pragma unroll
        for (int u = 0; u < 16; ++u) of[u] = __builtin_amdgcn_raw_buffer_load_b128(wsr, WS_OA + osrc + u * 1024, 0, 16);
        WAIT_BAR(0);
        STAMP(8);
        const lds_cptr wo = L + (256 * kh + 8 * (lane >> 5) + ((lane >> 2) & 3)) * 128 + ((ct ^ ((lane >> 3) & 1)) * 64) + (lg & 1) * 32 + (lane & 3) * 8;
        f32x16 acca = {}, accb = {};
#pragma unroll
        for (int u = 0; u < 16; ++u) { const u32x2 a = tr64(wo + WOA_OFF + u * 2048), b = tr64(wo + WOA_OFF + u * 2048 + 512); acca = mfma32((u32x4){a[0], a[1], b[0], b[1]}, of[u], acca); }
        STAMP(9);
        wait_flags((gu64*)(ws + WS_OBFLAG) + j * 32, 32, lane, tag);
        STAMP(10);
#pragma unroll
        for (int u = 0; u < 16; ++u) of[u] = __builtin_amdgcn_raw_buffer_load_b128(wsr, WS_OB + osrc + u * 1024, 0, 16);
        if (blockIdx.x == 0 && tid == 0) __hip_atomic_store((gu64*)(ws + WS_EPOCH), tag, RLX_AGENT);
#pragma unroll
        for (int u = 0; u < 16; ++u) { const u32x2 a = tr64(wo + WOB_OFF + u * 2048), b = tr64(wo + WOB_OFF + u * 2048 + 512); accb = mfma32((u32x4){a[0], a[1], b[0], b[1]}, of[u], accb); }
        const float gv = *(LAS const float*)(lds + GATE_OFF + (32 * tt + (lane & 31)) * 4);
        acca = acca * gv + accb * (1.f - gv);
        const f32x8 give = kh ? lo8(acca) : hi8(acca); f32x8 keep = kh ? hi8(acca) : lo8(acca);
        LAS f32x4* rw = (LAS f32x4*)(lds + RED_OFF + w * 2048 + lane * 16);
        rw[0] = __builtin_shufflevector(give, give, 0, 1, 2, 3); rw[64] = __builtin_shufflevector(give, give, 4, 5, 6, 7);
        asm volatile("s_waitcnt lgkmcnt(0)\n\ts_barrier" ::: "memory");
        LAS const f32x4* rr = (LAS const f32x4*)(lds + RED_OFF + (w ^ 4) * 2048 + lane * 16);
        const f32x4 r0 = rr[0], r1 = rr[64];
        const unsigned a0 = cvtpk(keep[0] + r0[0], keep[1] + r0[1]), a1 = cvtpk(keep[2] + r0[2], keep[3] + r0[3]), b0 = cvtpk(keep[4] + r1[0], keep[5] + r1[1]), b1 = cvtpk(keep[6] + r1[2], keep[7] + r1[3]);
        auto p0 = __builtin_amdgcn_permlane32_swap(a0, b0, false, false); auto p1 = __builtin_amdgcn_permlane32_swap(a1, b1, false, false);
        *(u32x4*)(out + (size_t)(64 * i + 32 * tt + (lane & 31)) * 1024 + g * 128 + ct * 64 + kh * 32 + (lane >> 5) * 16) = (u32x4){p0[0], p1[0], p0[1], p1[1]};
    }
    STAMP(11);
}
}

extern "C" void kernel_launch(void* const* d_in, const int* in_sizes, int n_in, void* d_out, int out_size, void* d_ws, size_t ws_size, hipStream_t stream) {
    fused_kernel<<<256, 512, 0, stream>>>((const char*)d_in[0], (const char*)d_in[1], (const char*)d_in[2], (const char*)d_in[4], (const char*)d_in[5],
                                          (const char*)d_in[6], (char*)d_out, (char*)d_ws);
}
```

```cpp
#include <hip/hip_runtime.h>
#include <hip/hip_bf16.h>
#include <stdint.h>

#define STAMPS 0

#define GAS __attribute__((address_space(1)))
#define LAS __attribute__((address_space(3)))
#define RLX_AGENT __ATOMIC_RELAXED, __HIP_MEMORY_SCOPE_AGENT
typedef unsigned u32x2 __attribute__((ext_vector_type(2)));
typedef unsigned u32x4 __attribute__((ext_vector_type(4)));
typedef float f32x2 __attribute__((ext_vector_type(2)));
typedef float f32x4 __attribute__((ext_vector_type(4)));
typedef float f32x8 __attribute__((ext_vector_type(8)));
typedef float f32x16 __attribute__((ext_vector_type(16)));
typedef __bf16 bf16x2 __attribute__((ext_vector_type(2)));
typedef __bf16 bf16x8 __attribute__((ext_vector_type(8)));
typedef __bf16 bf16x16 __attribute__((ext_vector_type(16)));
typedef short s16x4 __attribute__((ext_vector_type(4)));
typedef GAS unsigned long long gu64;
typedef LAS const char* lds_cptr;

namespace {
constexpr unsigned WS_EPOCH = 0;
constexpr unsigned WS_KVFLAG = 4096;
constexpr unsigned WS_OAFLAG = 16384;
constexpr unsigned WS_OBFLAG = 262144;
constexpr unsigned WS_GATE = 294912;
constexpr unsigned WS_STAMP = 32768;
constexpr unsigned WS_Q = 1u << 20;
constexpr unsigned WS_KV = WS_Q + (2u << 20);
constexpr unsigned WS_OA = WS_KV + (4u << 20);
constexpr unsigned WS_OB = WS_OA + (2u << 20);
constexpr unsigned WS_END = WS_OB + (2u << 20);
constexpr int X_OFF = 0, XKS = 80 * 128, WR_OFF = 8 * XKS, WSLOT = 24576;
constexpr int GW_OFF = WR_OFF + 3 * WSLOT, GATE_OFF = GW_OFF + 1024, ONES_OFF = 159744, LDS_BYTES = 163840;
constexpr int PROG_OFF = GATE_OFF + 256;
constexpr int SPARE_OFF = 131072;
static_assert(GATE_OFF + 512 <= ONES_OFF && SPARE_OFF + 8 * 3072 <= GW_OFF, "lds");
constexpr float QSCALE = 0.25f * 1.4426950408889634f;
constexpr unsigned kSpinCap = 1u << 18;

#define SBAR() __builtin_amdgcn_sched_barrier(0)
#define PIN(x) asm volatile("" : "+v"(x))

__device__ __forceinline__ unsigned cvtpk(float lo, float hi) { return __builtin_bit_cast(unsigned, __builtin_convertvector((f32x2){lo, hi}, bf16x2)); }
#define GLDS_OPEN "s_mov_b32 %0, m0\n\ts_mov_b32 m0, %3\n\ts_nop 2\n\t"
#define GLDS_ARGS : "=&s"(sv) : "v"(voff), "s"(sbase), "s"(ldsb) : "memory"
__device__ __forceinline__ void glds1(const void* sbase, unsigned voff, unsigned ldsb) { unsigned sv; asm volatile(GLDS_OPEN "global_load_lds_dwordx4 %1, %2\n\ts_mov_b32 m0, %0" GLDS_ARGS); }
__device__ __forceinline__ void glds3(const void* sbase, unsigned voff, unsigned ldsb) { unsigned sv;
    asm volatile(GLDS_OPEN "global_load_lds_dwordx4 %1, %2\n\tglobal_load_lds_dwordx4 %1, %2 offset:1024\n\tglobal_load_lds_dwordx4 %1, %2 offset:2048\n\ts_mov_b32 m0, %0" GLDS_ARGS); }
__device__ __forceinline__ void glds4c(const void* sbase, unsigned voff, unsigned ldsb) { unsigned sv;
    asm volatile(GLDS_OPEN "global_load_lds_dwordx4 %1, %2 sc1\n\tglobal_load_lds_dwordx4 %1, %2 offset:1024 sc1\n\tglobal_load_lds_dwordx4 %1, %2 offset:2048 sc1\n\tglobal_load_lds_dwordx4 %1, %2 offset:3072 sc1\n\ts_mov_b32 m0, %0" GLDS_ARGS); }
__device__ __forceinline__ void touch(const void* sbase, unsigned voff, unsigned ldsb) { unsigned sv; asm volatile(GLDS_OPEN "global_load_lds_dword %1, %2\n\ts_mov_b32 m0, %0" GLDS_ARGS); }
__device__ __forceinline__ void glds2c(const void* sbase, unsigned voff, unsigned ldsb) { unsigned sv;
    asm volatile(GLDS_OPEN "global_load_lds_dwordx4 %1, %2 sc1\n\tglobal_load_lds_dwordx4 %1, %2 offset:1024 sc1\n\ts_mov_b32 m0, %0" GLDS_ARGS); }
__device__ __forceinline__ u32x2 tr64(lds_cptr p) { return __builtin_bit_cast(u32x2, __builtin_amdgcn_ds_read_tr16_b64_v4i16((LAS s16x4*)p)); }
__device__ __forceinline__ u32x4 tr128(lds_cptr p) { const u32x2 a = tr64(p), b = tr64(p + 512); return (u32x4){a[0], a[1], b[0], b[1]}; }
__device__ __forceinline__ u32x4 lds128(lds_cptr p) { return *(LAS const u32x4*)p; }
__device__ __forceinline__ f32x4 mfma16(u32x4 a, u32x4 b, f32x4 c) { return __builtin_amdgcn_mfma_f32_16x16x32_bf16(__builtin_bit_cast(bf16x8, a), __builtin_bit_cast(bf16x8, b), c, 0, 0, 0); }
__device__ __forceinline__ f32x4 mfma16k16(u32x2 a, u32x2 b, f32x4 c) { return __builtin_amdgcn_mfma_f32_16x16x16bf16_1k(__builtin_bit_cast(s16x4, a), __builtin_bit_cast(s16x4, b), c, 0, 0, 0); }
__device__ __forceinline__ f32x16 mfma32(u32x4 a, u32x4 b, f32x16 c) { return __builtin_amdgcn_mfma_f32_32x32x16_bf16(__builtin_bit_cast(bf16x8, a), __builtin_bit_cast(bf16x8, b), c, 0, 0, 0); }
__device__ __forceinline__ f32x16 smf(u32x4 a, f32x8 b, f32x16 c, int idx) { return __builtin_amdgcn_smfmac_f32_32x32x32_bf16(__builtin_bit_cast(bf16x8, a), __builtin_bit_cast(bf16x16, b), c, idx, 0, 0); }
__device__ __forceinline__ u32x2 pack4(f32x4 v) { return (u32x2){cvtpk(v[0], v[1]), cvtpk(v[2], v[3])}; }
__device__ __forceinline__ u32x4 join16(u32x2 x, u32x2 y) {
    auto a = __builtin_amdgcn_permlane16_swap(x[0], y[0], false, false); auto b = __builtin_amdgcn_permlane16_swap(x[1], y[1], false, false);
    return (u32x4){a[0], b[0], a[1], b[1]}; }
__device__ __forceinline__ u32x4 join32(unsigned a0, unsigned a1, unsigned b0, unsigned b1) {
    auto p0 = __builtin_amdgcn_permlane32_swap(a0, b0, false, false); auto p1 = __builtin_amdgcn_permlane32_swap(a1, b1, false, false);
    return (u32x4){p0[0], p1[0], p0[1], p1[1]}; }
__device__ __forceinline__ float mx(float a, float b) { return __builtin_elementwise_maximum(a, b); }
__device__ __forceinline__ float xmax16(float v) { auto r = __builtin_amdgcn_permlane16_swap(__float_as_uint(v), __float_as_uint(v), false, false); return mx(__uint_as_float(r[0]), __uint_as_float(r[1])); }
__device__ __forceinline__ float xmax32(float v) { auto r = __builtin_amdgcn_permlane32_swap(__float_as_uint(v), __float_as_uint(v), false, false); return mx(__uint_as_float(r[0]), __uint_as_float(r[1])); }
__device__ __forceinline__ float xadd16(float v) { auto r = __builtin_amdgcn_permlane16_swap(__float_as_uint(v), __float_as_uint(v), false, false); return __uint_as_float(r[0]) + __uint_as_float(r[1]); }
__device__ __forceinline__ float xadd32(float v) { auto r = __builtin_amdgcn_permlane32_swap(__float_as_uint(v), __float_as_uint(v), false, false); return __uint_as_float(r[0]) + __uint_as_float(r[1]); }
__device__ __forceinline__ f32x8 lo8(const f32x16& v) { return __builtin_shufflevector(v, v, 0, 1, 2, 3, 4, 5, 6, 7); }
__device__ __forceinline__ f32x8 hi8(const f32x16& v) { return __builtin_shufflevector(v, v, 8, 9, 10, 11, 12, 13, 14, 15); }

__device__ __forceinline__ void wait_flags(gu64* f, int mask, int lane, unsigned long long tag) {
    gu64* p = f + (lane & mask);
#pragma unroll 1
    for (unsigned spins = 0; spins < kSpinCap; ++spins) {
        const unsigned long long v = __hip_atomic_load(p, RLX_AGENT);
        if (__all(v == tag)) break;
        __builtin_amdgcn_s_sleep(1);
    }
    __builtin_amdgcn_fence(__ATOMIC_ACQUIRE, "wavefront");
}

#define STAMP(k) do {} while (0)
#define TMS(s, c) do {} while (0)
#define STAMP_DUMP() do {} while (0)

template <int N> struct IC { static constexpr int v = N; };
template <int B, int E, class F> __device__ __forceinline__ void static_for(F&& f) { if constexpr (B < E) { f(IC<B>{}); static_for<B + 1, E>(f); } }
template <int N> __device__ __forceinline__ void wait_bar() { asm volatile("s_waitcnt vmcnt(%0) lgkmcnt(0)\n\ts_barrier" :: "i"(N) : "memory"); }
struct Frags { u32x4 x[2][5], w[2][3]; };

__global__ __launch_bounds__(512, 2) void fused_kernel(const char* __restrict__ x, const char* __restrict__ wqa, const char* __restrict__ woa,
                                                       const char* __restrict__ wqb, const char* __restrict__ wob, const char* __restrict__ gw,
                                                       char* __restrict__ out, char* ws) {
    __shared__ __attribute__((aligned(256))) char lds[LDS_BYTES];
    const int tid = threadIdx.x, lane = tid & 63, w = __builtin_amdgcn_readfirstlane(tid >> 6);
    const int xcd = blockIdx.x & 7, idx = blockIdx.x >> 3;
    const int g = 2 * (xcd >> 1) + (idx & 1), i = 16 * (xcd & 1) + (idx >> 1);
    const int h = 4 * xcd + (idx & 3), j = idx >> 2;
    const int i3 = 4 * xcd + (idx & 3), g3 = idx >> 2;
    const unsigned lds0 = (unsigned)(uintptr_t)(LAS char*)lds;
    const lds_cptr L = (lds_cptr)lds;
    const __amdgpu_buffer_rsrc_t wsr = __builtin_amdgcn_make_buffer_rsrc(ws, 0, WS_END, 0x00020000);
    const int lane16 = lane * 16, l15 = lane & 15, lg = lane >> 4, wq = w & 3;
#define LDC(soff) __builtin_amdgcn_raw_buffer_load_b128(wsr, lane16, (soff), 16)
#define STC(v, soff) __builtin_amdgcn_raw_buffer_store_b128((v), wsr, lane16 + (soff), 0, 16)
#define BAR() asm volatile("s_waitcnt lgkmcnt(0)\n\ts_barrier" ::: "memory")
#define VM0() asm volatile("s_waitcnt vmcnt(0)" ::: "memory")
    STAMP(0);
    const int rot = (i + 4 * (g & 1) + (g >> 1)) & 7;
#define KS(s) (((s) + rot) & 7)
#define EPLD(ep) asm volatile("s_nop 4\n\tglobal_load_dwordx2 %0, %1, %2 sc1" : "=v"(ep) : "v"(0), "s"(ws + WS_EPOCH) : "memory")
    auto mktag = [&](unsigned long long ep) { unsigned long long z = ep + 0x9E3779B97F4A7C15ull; z = (z ^ (z >> 30)) * 0xBF58476D1CE4E5B9ull; z = (z ^ (z >> 27)) * 0x94D049BB133111EBull; z ^= z >> 31; return ((z ^ ep) >> 32) ? z : ~z; };
    unsigned long long tag;
#define LA_AFTER 1
#define LPRIO 3
#define PUBS 8
#define PFKB 21
#define CODE_PREFETCH() do { if (PFKB) { const char* sbase = (const char*)__builtin_amdgcn_s_getpc(); sbase -= (uintptr_t)sbase & 127; const unsigned voff = ((idx * 4 + w) * 8 + (lane & 7)) % (PFKB * 8) * 128, ldsb = lds0 + GATE_OFF + 512; unsigned sv; \
        asm volatile(GLDS_OPEN "global_load_lds_dword %1, %2\n\ts_mov_b32 m0, %0" GLDS_ARGS); } } while (0)
    const int xsw = (l15 >> 1) & 7;
    const lds_cptr xrd0 = L + X_OFF + l15 * 128 + ((lg ^ xsw) * 16), xrd1 = L + X_OFF + l15 * 128 + (((lg + 4) ^ xsw) * 16);
    const unsigned xv0 = (lane >> 3) * 1024 + (((lane & 7) ^ lg) * 16), xv1 = (xv0 ^ 64) + 8192;
#define SETW(n, v) do { if (lane == 0) __hip_atomic_store((LAS int*)(lds + PROG_OFF) + (n), (v), __ATOMIC_RELAXED, __HIP_MEMORY_SCOPE_WORKGROUP); } while (0)
#define WAITW(n, v) do { while (!__all(__hip_atomic_load((LAS int*)(lds + PROG_OFF) + (n) + (lane & 3), __ATOMIC_RELAXED, __HIP_MEMORY_SCOPE_WORKGROUP) >= (v))) __builtin_amdgcn_s_sleep(1); asm volatile("" ::: "memory"); } while (0)
    if (w >= 4) {
        __builtin_amdgcn_s_setprio(LPRIO);
        const unsigned wv0 = (16 * wq + (lane >> 3)) * 3072 + g * 128 + (((lane & 7) ^ ((lg & 1) << 1)) * 16), wv1 = (wv0 ^ 64) + 8 * 3072;
        const unsigned wdst = lds0 + WR_OFF + wq * 6144;
        auto issue = [&](auto S) { constexpr int s = S.v;
            if constexpr (s == 8 || s == 9) {
                const int st = 4 * (s - 8) + wq; const char* hs = x + (size_t)(i ? 64 * i - 16 : 0) * 1024 + KS(st) * 128;
                glds1(hs, xv0, lds0 + X_OFF + st * XKS); glds1(hs, xv1, lds0 + X_OFF + st * XKS + 1024); }
            const char* wsrc = (s < 8 ? wqb : wqa) + (size_t)(KS(s) * 64 * 3072);
            glds3(wsrc, wv0, wdst + (s % 3) * WSLOT); glds3(wsrc, wv1, wdst + (s % 3) * WSLOT + 3072); };
        issue(IC<0>{}); issue(IC<1>{});
        wait_bar<6>();
        unsigned long long ep; EPLD(ep);
        if (w == 4) glds1(gw, lane16, lds0 + GW_OFF);
        issue(IC<2>{});
        f32x4 ga = {};
        const lds_cptr gwrd = L + GW_OFF + lg * 16;
        const char* kvsrc = ws + WS_KV + h * 131072 + wq * 8192;
        gu64* kvf = (gu64*)(ws + WS_KVFLAG) + h * 32;
#define KVLD(b) do { glds4c(kvsrc + (b) * 32768, lane16, lds0 + (b) * 32768 + wq * 8192); glds4c(kvsrc + (b) * 32768 + 4096, lane16, lds0 + (b) * 32768 + wq * 8192 + 4096); } while (0)
        static_for<0, 14>([&](auto S) { constexpr int s = S.v;
            constexpr int cnt[14] = {7, 6, 6, 6, 6, 6, 8, 8, 6, 6, 6, 6, 6, 6};
            TMS(s, cnt[s]);
            wait_bar<cnt[s]>();
            u32x4 g0, g1, g2, g3;
            if constexpr (s >= 4 && s < 12) if (g == (i & 7)) { constexpr int c = s - 4;
                g0 = lds128(gwrd + KS(c) * 128); g1 = lds128(xrd0 + c * XKS + (1 + wq) * 2048); g2 = lds128(gwrd + KS(c) * 128 + 64); g3 = lds128(xrd1 + c * XKS + (1 + wq) * 2048); }
            if constexpr (s + 3 < 16) issue(IC<s + 3>{});
            if constexpr (s == 7) { PIN(ep); tag = mktag(ep); }
            if constexpr (s >= 4 && s < 12) if (g == (i & 7)) { ga = mfma16(g0, g1, ga); ga = mfma16(g2, g3, ga); }
                    });
        VM0(); SETW(4 + wq, 1);
        wait_flags(kvf, 31, lane, tag);
        STAMP(4);
        KVLD(0);
        WAITW(0, 1);
        glds2c(ws + WS_Q + (h * 64 + 8 * j + 2 * wq) * 1024, lane16, lds0 + SPARE_OFF + wq * 2048); KVLD(1);
        WAITW(0, 2);
        if (g == (i & 7) && lane < 16) __hip_atomic_store((gu64*)(ws + WS_GATE) + 64 * i + 16 * wq + lane, (tag & 0xffffffff00000000ull) | __float_as_uint(1.f / (1.f + __expf(-ga[0]))), RLX_AGENT);
        KVLD(2); KVLD(3);
        __builtin_amdgcn_s_setprio(0);
        STAMP(2); STAMP(3);
        wait_bar<16>();
    } else {
        CODE_PREFETCH();
        const char* xsrc = x + (size_t)(64 * i + 16 * w) * 1024; const unsigned xdst = lds0 + X_OFF + (16 + 16 * w) * 128;
#define XLD(s) do { glds1(xsrc + KS(s) * 128, xv0, xdst + (s) * XKS); glds1(xsrc + KS(s) * 128, xv1, xdst + (s) * XKS + 1024); } while (0)
        XLD(0); XLD(1); XLD(2); XLD(3);
        unsigned long long ep;
        { const unsigned ov = (l15 == 0 || l15 == 4) ? 0x3F803F80u : 0u; *(LAS u32x4*)(lds + ONES_OFF + tid * 16) = (u32x4){ov, ov, ov, ov}; }
        if (tid < 8) *(LAS int*)(lds + PROG_OFF + tid * 4) = 0;
        const lds_cptr wrd = L + WR_OFF + lg * 3072 + ((lane >> 2) & 3) * 128 + ((w ^ (((lane >> 3) & 1) | ((lg & 1) << 1))) * 32) + (lane & 3) * 8;
        auto readf = [&](auto S, Frags& f) { constexpr int s = S.v;
#pragma unroll
            for (int cc = 0; cc < 2; ++cc) {
#pragma unroll
                for (int sec = 0; sec < 3; ++sec) f.w[cc][sec] = tr128(wrd + (s % 3) * WSLOT + cc * 12288 + sec * 1024);
#pragma unroll
                for (int t = (s < 8 ? 1 : 0); t < 5; ++t) f.x[cc][t] = lds128((cc ? xrd1 : xrd0) + (s & 7) * XKS + t * 2048);
            } };
        f32x4 q[5] = {}, k[5] = {}, v[5] = {};
        Frags fr[2];
        wait_bar<6>();
        readf(IC<0>{}, fr[0]);
        auto mm = [&](auto S, const Frags& f) { constexpr int s = S.v;
#pragma unroll
            for (int cc = 0; cc < 2; ++cc)
#pragma unroll
                for (int t = (s < 8 ? 1 : 0); t < 5; ++t) { k[t] = mfma16(f.w[cc][1], f.x[cc][t], k[t]); v[t] = mfma16(f.x[cc][t], f.w[cc][2], v[t]); if (t) q[t] = mfma16(f.w[cc][0], f.x[cc][t], q[t]); } };
        static_for<0, 16>([&](auto S) { constexpr int s = S.v;
            constexpr int cnt[14] = {4, 4, 4, 4, 4, 3, 1, 63, 63, 63, 63, 63, 63, 63};
            if constexpr (s == 7) {
                mm(S, fr[1]);
                const unsigned dst = WS_KV + (4 * g + w) * 131072 + (i >> 1) * 8192 + (i & 1) * 2048;
#pragma unroll
                for (int t = 0; t < 2; ++t) {
                    STC(join16(pack4(k[1 + 2 * t]), pack4(k[2 + 2 * t])), dst + t * 1024);
                    STC(join16(pack4(v[1 + 2 * t]), pack4(v[2 + 2 * t])), dst + 4096 + t * 1024);
                    STC(join16(pack4(q[1 + 2 * t] * QSCALE), pack4(q[2 + 2 * t] * QSCALE)), WS_Q + ((4 * g + w) * 64 + 2 * i + t) * 1024); }
#pragma unroll
                for (int t = 0; t < 5; ++t) q[t] = k[t] = v[t] = (f32x4){};
                STAMP(1); SBAR();
            }
            if constexpr (s < 14) { TMS(s, cnt[s]); wait_bar<cnt[s]>(); }
            else { asm volatile("s_waitcnt lgkmcnt(0)" ::: "memory"); TMS(s, 63); SETW(w, s - 13); if constexpr (s == 14) WAITW(4, 1); }
            SBAR();
            if constexpr (s < 15) readf(IC<s + 1>{}, fr[(s + 1) & 1]);
            if constexpr (s != 7) mm(S, fr[s & 1]);
            if constexpr (s < 15 && s != 7) {
#pragma unroll
                for (int u = 0; u < 22; ++u) { __builtin_amdgcn_sched_group_barrier(0x8, 1, 0); __builtin_amdgcn_sched_group_barrier(0x100, 1, 0); }
                __builtin_amdgcn_sched_group_barrier(0x8, 8, 0); }
            SBAR();
            if constexpr (s < 4) XLD(s + 4);
            if constexpr (s == 4) EPLD(ep);
            if constexpr (s == PUBS) { VM0(); PIN(ep); tag = mktag(ep); if (lane == 0) __hip_atomic_store((gu64*)(ws + WS_KVFLAG) + (4 * g + w) * 32 + i, tag, RLX_AGENT); }
        });
        BAR();
        STAMP(2);
        u32x2 kf[5], vf[5], of[4];
        f32x4 mc, mp, mp0;
#pragma unroll
        for (int r = 0; r < 4; ++r) { const int dc = l15 - (4 * lg + r); mc[r] = (dc >= 0 && !(dc & 1)) ? 0.f : -3e38f; mp[r] = (dc <= -2 && !(dc & 1)) ? 0.f : -3e38f; mp0[r] = i ? mp[r] : -3e38f; }
#pragma unroll
        for (int t = 0; t < 5; ++t) { kf[t] = pack4(k[t]); vf[t] = pack4(v[t]); }
        const u32x2 one2 = {0x3F803F80u, 0x3F803F80u};
#pragma unroll
        for (int sb = 0; sb < 4; ++sb) {
            const f32x4 z = {}; const u32x2 qs = pack4(q[sb + 1] * QSCALE);
            f32x4 sp = mfma16k16(kf[sb], qs, sb ? mp : mp0), sc = mfma16k16(kf[sb + 1], qs, mc);
            float m = mx(mx(mx(mx(sc[0], sc[1]), sc[2]), mx(mx(sc[3], sp[0]), sp[1])), mx(sp[2], sp[3]));
            m = xmax32(xmax16(m));
#pragma unroll
            for (int r = 0; r < 4; ++r) { sc[r] = __builtin_amdgcn_exp2f(sc[r] - m); sp[r] = __builtin_amdgcn_exp2f(sp[r] - m); }
            const u32x2 pp = pack4(sp), pc = pack4(sc);
            f32x4 l = mfma16k16(one2, pp, z); l = mfma16k16(one2, pc, l);
            f32x4 o = mfma16k16(vf[sb], pp, z); o = mfma16k16(vf[sb + 1], pc, o);
            o *= __builtin_amdgcn_rcpf(l[0]);
            of[sb] = pack4(o);
        }
        STC(join16(of[0], of[1]), WS_OA + (2 * i * 32 + 4 * g + w) * 1024);
        STC(join16(of[2], of[3]), WS_OA + ((2 * i + 1) * 32 + 4 * g + w) * 1024);
        STAMP(3);
    }
    STAMP(5);
    const u32x4 qa = lds128(L + SPARE_OFF + wq * 2048 + lane16), qb = lds128(L + SPARE_OFF + wq * 2048 + 1024 + lane16);
    const int tt = w & 1, kq = w >> 1, kh = w >> 2;
    const unsigned osrc = ((2 * i3 + tt) * 32 + 8 * kq) * 1024;
    const unsigned ovoff = (lane >> 3) * 1024 + g3 * 128 + (((lane & 7) ^ (((lane >> 4) & 1) << 2)) * 16);
    u32x4 of[8];
    unsigned long long gg;
    f32x16 o;
    const int sidx = (int)0xDDDDDDDDu;
    const bool real = (lane & 16) == 0;
    {
        unsigned kba = lds0 + kh * 8192 + lane16, vba = lds0 + (real ? kh * 8192 + 4096 : ONES_OFF) + (lane >> 5) * 512 + l15 * 16;
        const int vstep = real ? 16384 : 0;
        f32x16 sa, sb, ca, cb, oa = {}, ob = {};
        u32x4 kf = lds128(L + lane16), va = lds128((lds_cptr)(uintptr_t)vba), vc = va;
        { const f32x16 zero = {}; sa = mfma32(kf, qa, zero); sb = mfma32(kf, qb, zero); kf = lds128((lds_cptr)(uintptr_t)kba);
          float ma = sa[0], mb = sb[0];
#pragma unroll
          for (int r = 1; r < 16; ++r) { ma = mx(ma, sa[r]); mb = mx(mb, sb[r]); }
          ma = xmax32(ma); mb = xmax32(mb);
#pragma unroll
          for (int r = 0; r < 16; ++r) { ca[r] = -ma; cb[r] = -mb; sb[r] = 0.f; } }
        PIN(ca); PIN(cb);
        sa = mfma32(kf, qa, ca);
        auto run = [&](int n) {
            asm volatile("s_waitcnt lgkmcnt(0)\n\ts_nop 11\n"
            "1:\n\t"
            "v_exp_f32 v32, v32\n\tv_exp_f32 v33, v33\n\tv_smfmac_f32_32x32x32_bf16 %[ob], %[vc], v[56:63], %[ix]\n\tds_read_b128 %[vc], %[vb] offset:256\n\tv_exp_f32 v34, v34\n\tv_exp_f32 v35, v35\n\tv_exp_f32 v36, v36\n\tv_exp_f32 v37, v37\n\tv_mfma_f32_32x32x16_bf16 v[48:63], %[kf], %[qb], %[cb]\n\tds_read_b128 %[kf], %[kb] offset:1024\n\tv_exp_f32 v38, v38\n\tv_exp_f32 v39, v39\n\tv_exp_f32 v40, v40\n\tv_exp_f32 v41, v41\n\ts_waitcnt lgkmcnt(2)\n\tv_smfmac_f32_32x32x32_bf16 %[oa], %[va], v[32:39], %[ix]\n\tv_exp_f32 v42, v42\n\tv_exp_f32 v43, v43\n\tv_exp_f32 v44, v44\n\tv_exp_f32 v45, v45\n\tv_exp_f32 v46, v46\n\tv_exp_f32 v47, v47\n\t"
            "v_exp_f32 v48, v48\n\tv_exp_f32 v49, v49\n\ts_waitcnt lgkmcnt(0)\n\tv_smfmac_f32_32x32x32_bf16 %[oa], %[vc], v[40:47], %[ix]\n\tv_exp_f32 v50, v50\n\tv_exp_f32 v51, v51\n\tv_exp_f32 v52, v52\n\tv_exp_f32 v53, v53\n\tv_mfma_f32_32x32x16_bf16 v[32:47], %[kf], %[qa], %[ca]\n\tv_exp_f32 v54, v54\n\tv_exp_f32 v55, v55\n\tv_exp_f32 v56, v56\n\tv_exp_f32 v57, v57\n\tv_smfmac_f32_32x32x32_bf16 %[ob], %[va], v[48:55], %[ix]\n\tds_read_b128 %[va], %[vb] offset:1024\n\tv_exp_f32 v58, v58\n\tv_exp_f32 v59, v59\n\tv_exp_f32 v60, v60\n\tv_exp_f32 v61, v61\n\tv_exp_f32 v62, v62\n\tv_exp_f32 v63, v63\n\t"
            "v_exp_f32 v32, v32\n\tv_exp_f32 v33, v33\n\tv_smfmac_f32_32x32x32_bf16 %[ob], %[vc], v[56:63], %[ix]\n\tds_read_b128 %[vc], %[vb] offset:1280\n\tv_exp_f32 v34, v34\n\tv_exp_f32 v35, v35\n\tv_exp_f32 v36, v36\n\tv_exp_f32 v37, v37\n\tv_mfma_f32_32x32x16_bf16 v[48:63], %[kf], %[qb], %[cb]\n\tds_read_b128 %[kf], %[kb] offset:2048\n\tv_exp_f32 v38, v38\n\tv_exp_f32 v39, v39\n\tv_exp_f32 v40, v40\n\tv_exp_f32 v41, v41\n\ts_waitcnt lgkmcnt(2)\n\tv_smfmac_f32_32x32x32_bf16 %[oa], %[va], v[32:39], %[ix]\n\tv_exp_f32 v42, v42\n\tv_exp_f32 v43, v43\n\tv_exp_f32 v44, v44\n\tv_exp_f32 v45, v45\n\tv_exp_f32 v46, v46\n\tv_exp_f32 v47, v47\n\t"
            "v_exp_f32 v48, v48\n\tv_exp_f32 v49, v49\n\ts_waitcnt lgkmcnt(0)\n\tv_smfmac_f32_32x32x32_bf16 %[oa], %[vc], v[40:47], %[ix]\n\tv_exp_f32 v50, v50\n\tv_exp_f32 v51, v51\n\tv_exp_f32 v52, v52\n\tv_exp_f32 v53, v53\n\tv_mfma_f32_32x32x16_bf16 v[32:47], %[kf], %[qa], %[ca]\n\tv_exp_f32 v54, v54\n\tv_exp_f32 v55, v55\n\tv_exp_f32 v56, v56\n\tv_exp_f32 v57, v57\n\tv_smfmac_f32_32x32x32_bf16 %[ob], %[va], v[48:55], %[ix]\n\tds_read_b128 %[va], %[vb] offset:2048\n\tv_exp_f32 v58, v58\n\tv_exp_f32 v59, v59\n\tv_exp_f32 v60, v60\n\tv_exp_f32 v61, v61\n\tv_exp_f32 v62, v62\n\tv_exp_f32 v63, v63\n\t"
            "v_exp_f32 v32, v32\n\tv_exp_f32 v33, v33\n\tv_smfmac_f32_32x32x32_bf16 %[ob], %[vc], v[56:63], %[ix]\n\tds_read_b128 %[vc], %[vb] offset:2304\n\tv_exp_f32 v34, v34\n\tv_exp_f32 v35, v35\n\tv_exp_f32 v36, v36\n\tv_exp_f32 v37, v37\n\tv_mfma_f32_32x32x16_bf16 v[48:63], %[kf], %[qb], %[cb]\n\tds_read_b128 %[kf], %[kb] offset:3072\n\tv_exp_f32 v38, v38\n\tv_exp_f32 v39, v39\n\tv_exp_f32 v40, v40\n\tv_exp_f32 v41, v41\n\ts_waitcnt lgkmcnt(2)\n\tv_smfmac_f32_32x32x32_bf16 %[oa], %[va], v[32:39], %[ix]\n\tv_exp_f32 v42, v42\n\tv_exp_f32 v43, v43\n\tv_exp_f32 v44, v44\n\tv_exp_f32 v45, v45\n\tv_exp_f32 v46, v46\n\tv_exp_f32 v47, v47\n\t"
            "v_exp_f32 v48, v48\n\tv_exp_f32 v49, v49\n\ts_waitcnt lgkmcnt(0)\n\tv_smfmac_f32_32x32x32_bf16 %[oa], %[vc], v[40:47], %[ix]\n\tv_exp_f32 v50, v50\n\tv_exp_f32 v51, v51\n\tv_exp_f32 v52, v52\n\tv_exp_f32 v53, v53\n\tv_mfma_f32_32x32x16_bf16 v[32:47], %[kf], %[qa], %[ca]\n\tv_exp_f32 v54, v54\n\tv_exp_f32 v55, v55\n\tv_exp_f32 v56, v56\n\tv_exp_f32 v57, v57\n\tv_smfmac_f32_32x32x32_bf16 %[ob], %[va], v[48:55], %[ix]\n\tds_read_b128 %[va], %[vb] offset:3072\n\tv_exp_f32 v58, v58\n\tv_exp_f32 v59, v59\n\tv_exp_f32 v60, v60\n\tv_exp_f32 v61, v61\n\tv_exp_f32 v62, v62\n\tv_exp_f32 v63, v63\n\t"
            "v_exp_f32 v32, v32\n\tv_exp_f32 v33, v33\n\tv_smfmac_f32_32x32x32_bf16 %[ob], %[vc], v[56:63], %[ix]\n\tds_read_b128 %[vc], %[vb] offset:3328\n\tv_exp_f32 v34, v34\n\tv_exp_f32 v35, v35\n\tv_exp_f32 v36, v36\n\tv_exp_f32 v37, v37\n\tv_mfma_f32_32x32x16_bf16 v[48:63], %[kf], %[qb], %[cb]\n\tds_read_b128 %[kf], %[kb] offset:16384\n\tv_exp_f32 v38, v38\n\tv_exp_f32 v39, v39\n\tv_exp_f32 v40, v40\n\tv_exp_f32 v41, v41\n\ts_waitcnt lgkmcnt(2)\n\tv_smfmac_f32_32x32x32_bf16 %[oa], %[va], v[32:39], %[ix]\n\tv_exp_f32 v42, v42\n\tv_exp_f32 v43, v43\n\tv_exp_f32 v44, v44\n\tv_exp_f32 v45, v45\n\tv_exp_f32 v46, v46\n\tv_exp_f32 v47, v47\n\t"
            "v_exp_f32 v48, v48\n\tv_exp_f32 v49, v49\n\ts_waitcnt lgkmcnt(0)\n\tv_smfmac_f32_32x32x32_bf16 %[oa], %[vc], v[40:47], %[ix]\n\tv_exp_f32 v50, v50\n\tv_exp_f32 v51, v51\n\tv_exp_f32 v52, v52\n\tv_exp_f32 v53, v53\n\tv_mfma_f32_32x32x16_bf16 v[32:47], %[kf], %[qa], %[ca]\n\tv_exp_f32 v54, v54\n\tv_exp_f32 v55, v55\n\tv_exp_f32 v56, v56\n\tv_exp_f32 v57, v57\n\tv_add_u32 %[vb], %[vb], %[vs]\n\tv_smfmac_f32_32x32x32_bf16 %[ob], %[va], v[48:55], %[ix]\n\tds_read_b128 %[va], %[vb] offset:0\n\tv_add_u32 %[kb], 0x4000, %[kb]\n\ts_sub_u32 %[n], %[n], 1\n\tv_exp_f32 v58, v58\n\tv_exp_f32 v59, v59\n\tv_exp_f32 v60, v60\n\tv_exp_f32 v61, v61\n\tv_exp_f32 v62, v62\n\tv_exp_f32 v63, v63\n\t"
            "s_cmp_lg_u32 %[n], 0\n\ts_cbranch_scc1 1b\n\ts_waitcnt lgkmcnt(0)\n\ts_nop 7"
            : "+{v[32:47]}"(sa), "+{v[48:63]}"(sb), [oa] "+v"(oa), [ob] "+v"(ob), [kf] "+v"(kf), [va] "+v"(va), [vc] "+v"(vc), [kb] "+v"(kba), [vb] "+v"(vba), [n] "+s"(n)
            : [qa] "v"(qa), [qb] "v"(qb), [ca] "v"(ca), [cb] "v"(cb), [vs] "v"(vstep), [ix] "v"(sidx) : "scc", "memory");
        };
        STAMP(12);
        run(4);
        STAMP(13);
        VM0();
        if (w < 4 && lane == 0) __hip_atomic_store((gu64*)(ws + WS_OAFLAG) + i * 32 + 4 * g + w, tag, RLX_AGENT);
        BAR();
        kf = lds128((lds_cptr)(uintptr_t)kba); va = lds128((lds_cptr)(uintptr_t)vba); sa = mfma32(kf, qa, ca);
        if (w < 4) {
#pragma unroll
            for (int u = 0; u < 16; ++u) glds1(woa + (size_t)(128 * w + 8 * u) * 1024, ovoff, lds0 + w * 16384 + u * 1024);
            if (w < 2) touch(wob + g3 * 128, (128 * (idx & 3) + 64 * w + lane) * 1024, lds0 + GATE_OFF + 512); }
        STAMP(14);
        run(w < 4 ? 3 : 1);
        STAMP(15);
#pragma unroll 1
        for (unsigned spins = 0; spins < kSpinCap; ++spins) {
            const unsigned long long f = __hip_atomic_load((gu64*)(ws + WS_OAFLAG) + i3 * 32 + 8 * kq + (lane & 7), RLX_AGENT);
            gg = __hip_atomic_load((gu64*)(ws + WS_GATE) + 64 * i3 + 32 * tt + (lane & 31), RLX_AGENT);
            if (__all(f == tag && (gg >> 32) == (tag >> 32))) break;
            __builtin_amdgcn_s_sleep(1); }
        __builtin_amdgcn_fence(__ATOMIC_ACQUIRE, "wavefront");
#pragma unroll
        for (int u = 0; u < 8; ++u) of[u] = LDC(WS_OA + osrc + u * 1024);
        STAMP(8);
        run(w < 4 ? 1 : 3);
        ob = smf(vc, hi8(sb), ob, sidx);
        const f32x16 give = kh ? oa : ob; o = kh ? ob : oa;
        LAS char* xw = lds + SPARE_OFF + w * 3072 + lane16;
        *(LAS f32x4*)xw = __builtin_shufflevector(give, give, 0, 1, 2, 3); *(LAS f32x4*)(xw + 1024) = __builtin_shufflevector(give, give, 4, 5, 6, 7); *(LAS float*)(xw + 2048) = give[8];
        STAMP(6);
        wait_bar<0>();
        LAS const char* xr = lds + SPARE_OFF + (w ^ 4) * 3072 + lane16;
        const f32x4 r0 = *(LAS const f32x4*)xr, r1 = *(LAS const f32x4*)(xr + 1024);
#pragma unroll
        for (int r = 0; r < 4; ++r) { o[r] += r0[r]; o[4 + r] += r1[r]; }
        o[8] += *(LAS const float*)(xr + 2048);
    }
    {
        float chk = o[8];
#pragma unroll
        for (int r = 0; r < 8; ++r) chk = fmaxf(chk, fabsf(o[r]) * 0x1p-30f);
        if (__builtin_expect(__any(!(o[8] < 0x1p90f && chk < 0x1p90f)), 0)) {
            const f32x16 zero = {}; float m = -3e38f; const u32x4 qf = kh ? qb : qa;
            const unsigned ksrc = WS_KV + h * 131072;
#pragma unroll 1
            for (int t = 0; t < 64; ++t) { f32x16 s = mfma32(LDC(ksrc + (t >> 2) * 8192 + (t & 3) * 1024), qf, zero);
#pragma unroll
                for (int r = 0; r < 16; ++r) m = fmaxf(m, s[r]); }
            m = xmax32(m); o = zero;
            const unsigned vl = (lane >> 5) * 512 + l15 * 16;
            const u32x4 ones = {0x3F803F80u, 0x3F803F80u, 0x3F803F80u, 0x3F803F80u};
#pragma unroll 1
            for (int t = 0; t < 64; ++t) { const unsigned ts = ksrc + (t >> 2) * 8192 + (t & 3) * 1024; f32x16 s = mfma32(LDC(ts), qf, zero);
                const u32x4 a = __builtin_amdgcn_raw_buffer_load_b128(wsr, vl, ts + 4096, 16), c = __builtin_amdgcn_raw_buffer_load_b128(wsr, vl + 256, ts + 4096, 16);
#pragma unroll
                for (int r = 0; r < 16; ++r) s[r] = __builtin_amdgcn_exp2f(s[r] - m);
                o = smf(real ? a : ones, lo8(s), o, sidx); o = smf(real ? c : ones, hi8(s), o, sidx); }
        }
        const float rl = __builtin_amdgcn_rcpf(o[8]);
        const int T = 8 * j + 2 * wq + kh;
        STC(join32(cvtpk(o[0] * rl, o[1] * rl), cvtpk(o[2] * rl, o[3] * rl), cvtpk(o[4] * rl, o[5] * rl), cvtpk(o[6] * rl, o[7] * rl)), WS_OB + (T * 32 + h) * 1024);
    }
    STAMP(7);
    {
        const lds_cptr wo0 = L + kq * 16384 + (8 * (lane >> 5) + ((lane >> 2) & 3)) * 128 + (((lane >> 3) & 1) * 64) + (lg & 1) * 32 + (lane & 3) * 8;
        const lds_cptr wo1 = L + kq * 16384 + (8 * (lane >> 5) + ((lane >> 2) & 3)) * 128 + ((1 ^ ((lane >> 3) & 1)) * 64) + (lg & 1) * 32 + (lane & 3) * 8;
        f32x16 a0 = {}, a1 = {}, b0 = {}, b1 = {};
#pragma unroll
        for (int u = 0; u < 4; ++u) { a0 = mfma32(tr128(wo0 + u * 2048), of[u], a0); a1 = mfma32(tr128(wo1 + u * 2048), of[u], a1); }
        SBAR();
        STAMP(21);
        VM0();
        STAMP(22);
        if (lane == 0) __hip_atomic_store((gu64*)(ws + WS_OBFLAG) + (8 * j + 2 * wq + kh) * 32 + h, tag, RLX_AGENT);
#pragma unroll
        for (int u = 0; u < 8; ++u) glds1(wob + (size_t)(64 * w + 8 * u) * 1024, ovoff, lds0 + 65536 + w * 8192 + u * 1024);
#pragma unroll
        for (int u = 4; u < 8; ++u) { a0 = mfma32(tr128(wo0 + u * 2048), of[u], a0); a1 = mfma32(tr128(wo1 + u * 2048), of[u], a1); }
        const float gv = __uint_as_float((unsigned)gg);
        a0 *= gv; a1 *= gv; PIN(a0); PIN(a1);
        STAMP(9);
        wait_flags((gu64*)(ws + WS_OBFLAG) + (2 * i3 + tt) * 32 + 8 * kq, 7, lane, tag);
        STAMP(16);
#pragma unroll
        for (int u = 0; u < 8; ++u) of[u] = LDC(WS_OB + osrc + u * 1024);
        STAMP(10);
        asm volatile("s_barrier" ::: "memory");
        u32x4 wf[16];
#pragma unroll
        for (int u = 0; u < 8; ++u) { wf[2 * u] = tr128(wo0 + 65536 + u * 2048); wf[2 * u + 1] = tr128(wo1 + 65536 + u * 2048); }
        SBAR();
        STAMP(17);
#pragma unroll
        for (int u = 0; u < 8; ++u) { b0 = mfma32(wf[2 * u], of[u], b0); b1 = mfma32(wf[2 * u + 1], of[u], b1); }
        a0 += b0 * (1.f - gv); a1 += b1 * (1.f - gv);
        STAMP(18);
        LAS f32x4* rw = (LAS f32x4*)(lds + tt * 32768 + kq * 2048 + lane16);
        rw[0] = __builtin_shufflevector(a0, a0, 0, 1, 2, 3); rw[64] = __builtin_shufflevector(a0, a0, 4, 5, 6, 7);
        rw[512] = __builtin_shufflevector(a0, a0, 8, 9, 10, 11); rw[576] = __builtin_shufflevector(a0, a0, 12, 13, 14, 15);
        rw[1024] = __builtin_shufflevector(a1, a1, 0, 1, 2, 3); rw[1088] = __builtin_shufflevector(a1, a1, 4, 5, 6, 7);
        rw[1536] = __builtin_shufflevector(a1, a1, 8, 9, 10, 11); rw[1600] = __builtin_shufflevector(a1, a1, 12, 13, 14, 15);
        STAMP(19);
        BAR();
        STAMP(20);
        if (blockIdx.x == 0 && tid == 0) __hip_atomic_store((gu64*)(ws + WS_EPOCH), tag, RLX_AGENT);
        LAS const f32x4* rr = (LAS const f32x4*)(lds + tt * 32768 + kq * 8192 + lane16);
        const f32x4 r0 = (rr[0] + rr[128]) + (rr[256] + rr[384]), r1 = (rr[64] + rr[192]) + (rr[320] + rr[448]);
        *(u32x4*)(out + (size_t)(64 * i3 + 32 * tt + (lane & 31)) * 1024 + g3 * 128 + kq * 32 + (lane >> 5) * 16) =
            join32(cvtpk(r0[0], r0[1]), cvtpk(r0[2], r0[3]), cvtpk(r1[0], r1[1]), cvtpk(r1[2], r1[3]));
    }
    STAMP(11);
    STAMP_DUMP();
}
}

extern "C" void kernel_launch(void* const* d_in, const int* in_sizes, int n_in, void* d_out, int out_size, void* d_ws, size_t ws_size, hipStream_t stream) {
    fused_kernel<<<256, 512, 0, stream>>>((const char*)d_in[0], (const char*)d_in[1], (const char*)d_in[2], (const char*)d_in[4], (const char*)d_in[5],
                                          (const char*)d_in[6], (char*)d_out, (char*)d_ws);
}
```

```cpp
#include <hip/hip_runtime.h>
#include <hip/hip_bf16.h>
#include <stdint.h>

#define STAMPS 0

#define GAS __attribute__((address_space(1)))
#define LAS __attribute__((address_space(3)))
#define RLX_AGENT __ATOMIC_RELAXED, __HIP_MEMORY_SCOPE_AGENT
typedef unsigned u32x2 __attribute__((ext_vector_type(2)));
typedef unsigned u32x4 __attribute__((ext_vector_type(4)));
typedef float f32x2 __attribute__((ext_vector_type(2)));
typedef float f32x4 __attribute__((ext_vector_type(4)));
typedef float f32x8 __attribute__((ext_vector_type(8)));
typedef float f32x16 __attribute__((ext_vector_type(16)));
typedef __bf16 bf16x2 __attribute__((ext_vector_type(2)));
typedef __bf16 bf16x8 __attribute__((ext_vector_type(8)));
typedef __bf16 bf16x16 __attribute__((ext_vector_type(16)));
typedef short s16x4 __attribute__((ext_vector_type(4)));
typedef GAS unsigned long long gu64;
typedef LAS const char* lds_cptr;

#define SEG3 1
#define HSH 4
#define PSL 1
#define ONEPOLL 0
#define FSTRIDE 16
namespace {
constexpr unsigned WS_EPOCH = 0;
constexpr int FS = FSTRIDE;
constexpr unsigned WS_KVFLAG = 11u << 20;
constexpr unsigned WS_OAFLAG = WS_KVFLAG + 1024 * FS * 8;
constexpr unsigned WS_OBFLAG = WS_OAFLAG + 1024 * FS * 8;
constexpr unsigned WS_GATE = 294912;
constexpr unsigned WS_STAMP = 32768;
constexpr unsigned WS_Q = 1u << 20;
constexpr unsigned WS_KV = WS_Q + (2u << 20);
constexpr unsigned WS_OA = WS_KV + (4u << 20);
constexpr unsigned WS_OB = WS_OA + (2u << 20);
constexpr unsigned WS_END = WS_OB + (2u << 20);
static_assert(WS_END <= WS_KVFLAG, "ws");
constexpr int X_OFF = 0, XKS = 80 * 128, WR_OFF = 8 * XKS, WSLOT = 24576;
constexpr int GW_OFF = WR_OFF + 3 * WSLOT, GATE_OFF = GW_OFF + 1024, ONES_OFF = 159744, LDS_BYTES = 163840;
constexpr int PROG_OFF = GATE_OFF + 256, EP_OFF = GATE_OFF;
constexpr int SPARE_OFF = 131072;
static_assert(GATE_OFF + 512 <= ONES_OFF && SPARE_OFF + 8 * 3072 <= GW_OFF, "lds");
constexpr float QSCALE = 0.25f * 1.4426950408889634f;
constexpr unsigned kSpinCap = 1u << 18;

#define SBAR() __builtin_amdgcn_sched_barrier(0)
#define PIN(x) asm volatile("" : "+v"(x))

__device__ __forceinline__ unsigned cvtpk(float lo, float hi) { return __builtin_bit_cast(unsigned, __builtin_convertvector((f32x2){lo, hi}, bf16x2)); }
#define GLDS_OPEN "s_mov_b32 %0, m0\n\ts_mov_b32 m0, %3\n\ts_nop 2\n\t"
#define GLDS_ARGS : "=&s"(sv) : "v"(voff), "s"(sbase), "s"(ldsb) : "memory"
__device__ __forceinline__ void glds1(const void* sbase, unsigned voff, unsigned ldsb) { unsigned sv; asm volatile(GLDS_OPEN "global_load_lds_dwordx4 %1, %2\n\ts_mov_b32 m0, %0" GLDS_ARGS); }
__device__ __forceinline__ void glds3(const void* sbase, unsigned voff, unsigned ldsb) { unsigned sv;
    asm volatile(GLDS_OPEN "global_load_lds_dwordx4 %1, %2\n\tglobal_load_lds_dwordx4 %1, %2 offset:1024\n\tglobal_load_lds_dwordx4 %1, %2 offset:2048\n\ts_mov_b32 m0, %0" GLDS_ARGS); }
__device__ __forceinline__ void glds4c(const void* sbase, unsigned voff, unsigned ldsb) { unsigned sv;
    asm volatile(GLDS_OPEN "global_load_lds_dwordx4 %1, %2 sc1\n\tglobal_load_lds_dwordx4 %1, %2 offset:1024 sc1\n\tglobal_load_lds_dwordx4 %1, %2 offset:2048 sc1\n\tglobal_load_lds_dwordx4 %1, %2 offset:3072 sc1\n\ts_mov_b32 m0, %0" GLDS_ARGS); }
__device__ __forceinline__ void touch(const void* sbase, unsigned voff, unsigned ldsb) { unsigned sv; asm volatile(GLDS_OPEN "global_load_lds_dword %1, %2\n\ts_mov_b32 m0, %0" GLDS_ARGS); }
__device__ __forceinline__ void glds2c(const void* sbase, unsigned voff, unsigned ldsb) { unsigned sv;
    asm volatile(GLDS_OPEN "global_load_lds_dwordx4 %1, %2 sc1\n\tglobal_load_lds_dwordx4 %1, %2 offset:1024 sc1\n\ts_mov_b32 m0, %0" GLDS_ARGS); }
__device__ __forceinline__ u32x2 tr64(lds_cptr p) { return __builtin_bit_cast(u32x2, __builtin_amdgcn_ds_read_tr16_b64_v4i16((LAS s16x4*)p)); }
__device__ __forceinline__ u32x4 tr128(lds_cptr p) { const u32x2 a = tr64(p), b = tr64(p + 512); return (u32x4){a[0], a[1], b[0], b[1]}; }
__device__ __forceinline__ u32x4 lds128(lds_cptr p) { return *(LAS const u32x4*)p; }
__device__ __forceinline__ f32x4 mfma16(u32x4 a, u32x4 b, f32x4 c) { return __builtin_amdgcn_mfma_f32_16x16x32_bf16(__builtin_bit_cast(bf16x8, a), __builtin_bit_cast(bf16x8, b), c, 0, 0, 0); }
__device__ __forceinline__ f32x4 mfma16k16(u32x2 a, u32x2 b, f32x4 c) { return __builtin_amdgcn_mfma_f32_16x16x16bf16_1k(__builtin_bit_cast(s16x4, a), __builtin_bit_cast(s16x4, b), c, 0, 0, 0); }
__device__ __forceinline__ f32x16 mfma32(u32x4 a, u32x4 b, f32x16 c) { return __builtin_amdgcn_mfma_f32_32x32x16_bf16(__builtin_bit_cast(bf16x8, a), __builtin_bit_cast(bf16x8, b), c, 0, 0, 0); }
__device__ __forceinline__ f32x16 smf(u32x4 a, f32x8 b, f32x16 c, int idx) { return __builtin_amdgcn_smfmac_f32_32x32x32_bf16(__builtin_bit_cast(bf16x8, a), __builtin_bit_cast(bf16x16, b), c, idx, 0, 0); }
__device__ __forceinline__ u32x2 pack4(f32x4 v) { return (u32x2){cvtpk(v[0], v[1]), cvtpk(v[2], v[3])}; }
__device__ __forceinline__ u32x4 join16(u32x2 x, u32x2 y) {
    auto a = __builtin_amdgcn_permlane16_swap(x[0], y[0], false, false); auto b = __builtin_amdgcn_permlane16_swap(x[1], y[1], false, false);
    return (u32x4){a[0], b[0], a[1], b[1]}; }
__device__ __forceinline__ u32x4 join32(unsigned a0, unsigned a1, unsigned b0, unsigned b1) {
    auto p0 = __builtin_amdgcn_permlane32_swap(a0, b0, false, false); auto p1 = __builtin_amdgcn_permlane32_swap(a1, b1, false, false);
    return (u32x4){p0[0], p1[0], p0[1], p1[1]}; }
__device__ __forceinline__ float mx(float a, float b) { return __builtin_elementwise_maximum(a, b); }
__device__ __forceinline__ float xmax16(float v) { auto r = __builtin_amdgcn_permlane16_swap(__float_as_uint(v), __float_as_uint(v), false, false); return mx(__uint_as_float(r[0]), __uint_as_float(r[1])); }
__device__ __forceinline__ float xmax32(float v) { auto r = __builtin_amdgcn_permlane32_swap(__float_as_uint(v), __float_as_uint(v), false, false); return mx(__uint_as_float(r[0]), __uint_as_float(r[1])); }
__device__ __forceinline__ float xadd16(float v) { auto r = __builtin_amdgcn_permlane16_swap(__float_as_uint(v), __float_as_uint(v), false, false); return __uint_as_float(r[0]) + __uint_as_float(r[1]); }
__device__ __forceinline__ float xadd32(float v) { auto r = __builtin_amdgcn_permlane32_swap(__float_as_uint(v), __float_as_uint(v), false, false); return __uint_as_float(r[0]) + __uint_as_float(r[1]); }
__device__ __forceinline__ f32x8 lo8(const f32x16& v) { return __builtin_shufflevector(v, v, 0, 1, 2, 3, 4, 5, 6, 7); }
__device__ __forceinline__ f32x8 hi8(const f32x16& v) { return __builtin_shufflevector(v, v, 8, 9, 10, 11, 12, 13, 14, 15); }

__device__ __forceinline__ void wait_flags(gu64* f, int mask, int lane, unsigned long long tag) {
    gu64* p = f + (lane & mask) * FS;
#pragma unroll 1
    for (unsigned spins = 0; spins < kSpinCap; ++spins) {
        const unsigned long long v = __hip_atomic_load(p, RLX_AGENT);
        if (__all(v == tag)) break;
        __builtin_amdgcn_s_sleep(PSL);
    }
    __builtin_amdgcn_fence(__ATOMIC_ACQUIRE, "wavefront");
}

#define STAMP(k) do {} while (0)
#define TMS(s, c) do {} while (0)
#define STAMP_DUMP() do {} while (0)

template <int N> struct IC { static constexpr int v = N; };
template <int B, int E, class F> __device__ __forceinline__ void static_for(F&& f) { if constexpr (B < E) { f(IC<B>{}); static_for<B + 1, E>(f); } }
template <int N> __device__ __forceinline__ void wait_bar() { asm volatile("s_waitcnt vmcnt(%0) lgkmcnt(0)\n\ts_barrier" :: "i"(N) : "memory"); }
struct Frags { u32x4 x[2][5], w[2][3]; };

__global__ __launch_bounds__(512, 2) void fused_kernel(const char* __restrict__ x, const char* __restrict__ wqa, const char* __restrict__ woa,
                                                       const char* __restrict__ wqb, const char* __restrict__ wob, const char* __restrict__ gw,
                                                       char* __restrict__ out, char* ws) {
    __shared__ __attribute__((aligned(256))) char lds[LDS_BYTES];
    const int tid = threadIdx.x, lane = tid & 63, w = __builtin_amdgcn_readfirstlane(tid >> 6);
    const int xcd = blockIdx.x & 7, idx = blockIdx.x >> 3;
    const int g = 2 * (xcd >> 1) + (idx & 1), i = 16 * (xcd & 1) + (idx >> 1);
    const int h = 4 * ((xcd + HSH) & 7) + (idx & 3), j = idx >> 2;
    const int i3 = 4 * xcd + (idx & 3), g3 = idx >> 2;
    const unsigned lds0 = (unsigned)(uintptr_t)(LAS char*)lds;
    const lds_cptr L = (lds_cptr)lds;
    const __amdgpu_buffer_rsrc_t wsr = __builtin_amdgcn_make_buffer_rsrc(ws, 0, WS_END, 0x00020000);
    const int lane16 = lane * 16, l15 = lane & 15, lg = lane >> 4, wq = w & 3;
#define LDC(soff) __builtin_amdgcn_raw_buffer_load_b128(wsr, lane16, (soff), 16)
#define STC(v, soff) __builtin_amdgcn_raw_buffer_store_b128((v), wsr, lane16 + (soff), 0, 16)
#define BAR() asm volatile("s_waitcnt lgkmcnt(0)\n\ts_barrier" ::: "memory")
#define VM0() asm volatile("s_waitcnt vmcnt(0)" ::: "memory")
    STAMP(0);
    const int rot = (i + 4 * (g & 1) + (g >> 1)) & 7;
#define KS(s) (((s) + rot) & 7)
    auto mktag = [&](unsigned long long ev) { const unsigned long long ep = ((unsigned long long)__builtin_amdgcn_readfirstlane((unsigned)(ev >> 32)) << 32) | (unsigned)__builtin_amdgcn_readfirstlane((unsigned)ev);
        unsigned long long z = (ep + 0x9E3779B97F4A7C15ull) * 0xBF58476D1CE4E5B9ull; z ^= z >> 29; return ((z ^ ep) >> 32) ? z : ~z; };
    unsigned long long tag;
#define TCHB 0
#define TCH 1
#define EPOLL 0
#define KVOLD 1
#define SPF -1
#define LPRIO 3
#define PUBS 9
#define PFKB 21
#define CODE_PREFETCH() do { if (PFKB) { const char* sbase = (const char*)__builtin_amdgcn_s_getpc(); sbase -= (uintptr_t)sbase & 127; const unsigned voff = ((idx * 8 + w) * 4 + (lane & 3)) % (PFKB * 8) * 128, ldsb = lds0 + GATE_OFF + 512; unsigned sv; \
        asm volatile(GLDS_OPEN "global_load_lds_dword %1, %2\n\ts_mov_b32 m0, %0" GLDS_ARGS); } } while (0)
    const int xsw = (l15 >> 1) & 7;
    const lds_cptr xrd0 = L + X_OFF + l15 * 128 + ((lg ^ xsw) * 16), xrd1 = L + X_OFF + l15 * 128 + (((lg + 4) ^ xsw) * 16);
    const unsigned xv0 = (lane >> 3) * 1024 + (((lane & 7) ^ lg) * 16), xv1 = (xv0 ^ 64) + 8192;
    const unsigned wvl = (lane >> 3) * 3072 + g * 128 + (((lane & 7) ^ ((lg & 1) << 1)) * 16);
    auto first = [&](int s) { glds3(wqb + (size_t)((KS(s) * 64 + 8 * w) * 3072), wvl ^ ((w & 1) * 64), lds0 + WR_OFF + s * WSLOT + w * 3072);
                              glds1(x + (size_t)(64 * i + 8 * w) * 1024 + KS(s) * 128, xv0 ^ ((w & 1) * 64), lds0 + X_OFF + s * XKS + (16 + 8 * w) * 128); };
    if (TCHB) touch(wqb + blockIdx.x * 6144, (w * 6 + (lane < 6 ? lane : 5)) * 128, lds0 + GATE_OFF + 512);
    if (TCHB > 1) touch(x + blockIdx.x * 8192, (w * 8 + (lane & 7)) * 128, lds0 + GATE_OFF + 512);
    first(0);
    CODE_PREFETCH();
    first(1);
    if (TCH) touch(wqa + blockIdx.x * 6144, (w * 6 + (lane < 6 ? lane : 5)) * 128, lds0 + GATE_OFF + 512);
#define SETW(n, v) do { if (lane == 0) __hip_atomic_store((LAS int*)(lds + PROG_OFF) + (n), (v), __ATOMIC_RELAXED, __HIP_MEMORY_SCOPE_WORKGROUP); } while (0)
#define WAITW(n, v) do { while (!__all(__hip_atomic_load((LAS int*)(lds + PROG_OFF) + (n) + (lane & 3), __ATOMIC_RELAXED, __HIP_MEMORY_SCOPE_WORKGROUP) >= (v))) __builtin_amdgcn_s_sleep(1); asm volatile("" ::: "memory"); } while (0)
    const char* kvsrc = ws + WS_KV + h * 131072 + wq * 8192;
#define KVLD(b) do { glds4c(kvsrc + (b) * 32768, lane16, lds0 + (b) * 32768 + wq * 8192); glds4c(kvsrc + (b) * 32768 + 4096, lane16, lds0 + (b) * 32768 + wq * 8192 + 4096); } while (0)
    if (w >= 4) {
        __builtin_amdgcn_s_setprio(LPRIO);
        const unsigned wv0 = wvl + 16 * wq * 3072, wv1 = (wv0 ^ 64) + 8 * 3072;
        const unsigned wdst = lds0 + WR_OFF + wq * 6144;
        auto issue = [&](auto S) { constexpr int s = S.v;
            if constexpr (s == 8 || s == 9) {
                const int st = 4 * (s - 8) + wq; const char* hs = x + (size_t)(i ? 64 * i - 16 : 0) * 1024 + KS(st) * 128;
                glds1(hs, xv0, lds0 + X_OFF + st * XKS); glds1(hs, xv1, lds0 + X_OFF + st * XKS + 1024); }
            const char* wsrc = (s < 8 ? wqb : wqa) + (size_t)(KS(s) * 64 * 3072);
            glds3(wsrc, wv0, wdst + (s % 3) * WSLOT); glds3(wsrc, wv1, wdst + (s % 3) * WSLOT + 3072); };
        wait_bar<5 + TCH>();
        if (w == 4) glds1(gw, lane16, lds0 + GW_OFF);
        if (w == 5) { const void* sbase = ws + WS_EPOCH; const unsigned voff = (lane & 1) * 4, ldsb = lds0 + EP_OFF; unsigned sv; asm volatile(GLDS_OPEN "global_load_lds_dword %1, %2 sc1\n\ts_mov_b32 m0, %0" GLDS_ARGS); }
        issue(IC<2>{});
        f32x4 ga = {};
        unsigned sink; unsigned long long fl = 0;
        const lds_cptr gwrd = L + GW_OFF + lg * 16;
        gu64* kvf = (gu64*)(ws + WS_KVFLAG) + h * 32 * FS;
        static_for<0, 14>([&](auto S) { constexpr int s = S.v;
            constexpr int cnt[14] = {6, 6, 6, 6, 6, 6, 8, 63, 6, 6, 6, 6, 6, 6 + EPOLL};
            TMS(s, cnt[s]);
            if constexpr (SPF >= 0 && s > SPF && s < 4) asm volatile("s_waitcnt vmcnt(%0)\n\ts_barrier" :: "i"(cnt[s]) : "memory"); else wait_bar<cnt[s]>();
            if constexpr (SPF >= 0 && s == 4) asm volatile("" :: "s"(sink));
            u32x4 g0, g1, g2, g3;
            if constexpr (s >= 4 && s < 12) if (g == (i & 7)) { constexpr int c = s - 4;
                g0 = lds128(gwrd + KS(c) * 128); g1 = lds128(xrd0 + c * XKS + (1 + wq) * 2048); g2 = lds128(gwrd + KS(c) * 128 + 64); g3 = lds128(xrd1 + c * XKS + (1 + wq) * 2048); }
            if constexpr (s + 3 < 16) issue(IC<s + 3>{});
            if constexpr (s == 12 && EPOLL) asm volatile("s_nop 4\n\tglobal_load_dwordx2 %0, %1, %2 sc1" : "=v"(fl) : "v"((lane & 31) * 8 * FS), "s"(kvf) : "memory");
            if constexpr (s == SPF) {
                asm volatile(".set pfo, 0\n\t.rept 24\n\ts_load_dword %0, %1, pfo\n\t.set pfo, pfo + 1024\n\t.endr" : "=&s"(sink) : "s"(wqa + g * 128 + ((i & 15) * 4 + wq) * 24576) : "memory"); }
            if constexpr (s == 7) tag = mktag(*(LAS const unsigned long long*)(lds + EP_OFF));
            if constexpr (s >= 4 && s < 12) if (g == (i & 7)) { ga = mfma16(g0, g1, ga); ga = mfma16(g2, g3, ga); }
            if constexpr (s == 7) wait_bar<14>();
                    });
        VM0(); SETW(4 + wq, 1);
        if (EPOLL) PIN(fl);
        if (ONEPOLL) { wait_flags(kvf + wq * 8 * FS, 7, lane, tag); SETW(4 + wq, 2); WAITW(4, 2); }
        else if (!EPOLL || !__all(fl == tag)) wait_flags(kvf, 31, lane, tag);
        __builtin_amdgcn_fence(__ATOMIC_ACQUIRE, "wavefront");
        STAMP(4);
        KVLD(0);
        WAITW(0, 1);
        glds2c(ws + WS_Q + (h * 64 + 8 * j + 2 * wq) * 1024, lane16, lds0 + SPARE_OFF + wq * 2048); KVLD(1);
        STAMP(2);
        wait_bar<0>();
        if (!KVOLD) { KVLD(2); KVLD(3); }
        if (g == (i & 7) && lane < 16) __hip_atomic_store((gu64*)(ws + WS_GATE) + 64 * i + 16 * wq + lane, (tag & 0xffffffff00000000ull) | __float_as_uint(1.f / (1.f + __expf(-ga[0]))), RLX_AGENT);
        STAMP(3);
    } else {
        const char* xsrc = x + (size_t)(64 * i + 16 * w) * 1024; const unsigned xdst = lds0 + X_OFF + (16 + 16 * w) * 128;
#define XLD(s) do { glds1(xsrc + KS(s) * 128, xv0, xdst + (s) * XKS); glds1(xsrc + KS(s) * 128, xv1, xdst + (s) * XKS + 1024); } while (0)
        { const unsigned ov = (l15 == 0 || l15 == 4) ? 0x3F803F80u : 0u; *(LAS u32x4*)(lds + ONES_OFF + tid * 16) = (u32x4){ov, ov, ov, ov}; }
        if (tid < 24) *(LAS int*)(lds + PROG_OFF + tid * 4) = 0;
        const lds_cptr wrd = L + WR_OFF + lg * 3072 + ((lane >> 2) & 3) * 128 + ((w ^ (((lane >> 3) & 1) | ((lg & 1) << 1))) * 32) + (lane & 3) * 8;
        auto readf = [&](auto S, Frags& f) { constexpr int s = S.v;
#pragma unroll
            for (int cc = 0; cc < 2; ++cc) {
#pragma unroll
                for (int sec = 0; sec < 3; ++sec) f.w[cc][sec] = tr128(wrd + (s % 3) * WSLOT + cc * 12288 + sec * 1024);
#pragma unroll
                for (int t = (s < 8 ? 1 : 0); t < 5; ++t) f.x[cc][t] = lds128((cc ? xrd1 : xrd0) + (s & 7) * XKS + t * 2048);
            } };
        f32x4 q[5] = {}, k[5] = {}, v[5] = {};
        Frags fr[2];
        wait_bar<5 + TCH>();
        XLD(2); XLD(3);
        readf(IC<0>{}, fr[0]);
        auto mm = [&](auto S, const Frags& f) { constexpr int s = S.v;
#pragma unroll
            for (int cc = 0; cc < 2; ++cc)
#pragma unroll
                for (int t = (s < 8 ? 1 : 0); t < 5; ++t) { k[t] = mfma16(f.w[cc][1], f.x[cc][t], k[t]); v[t] = mfma16(f.x[cc][t], f.w[cc][2], v[t]); if (t) q[t] = mfma16(f.w[cc][0], f.x[cc][t], q[t]); } };
        static_for<0, 16>([&](auto S) { constexpr int s = S.v;
            constexpr int cnt[14] = {4 + TCH, 4, 4, 4, 4, 2, 0, 63, 63, 63, 63, 63, 63, 63};
            if constexpr (s == 7) {
                BAR(); SBAR();
                mm(S, fr[1]);
                const unsigned dst = WS_KV + (4 * g + w) * 131072 + (i >> 1) * 8192 + (i & 1) * 2048;
#pragma unroll
                for (int t = 0; t < 2; ++t) {
                    STC(join16(pack4(k[1 + 2 * t]), pack4(k[2 + 2 * t])), dst + t * 1024);
                    STC(join16(pack4(v[1 + 2 * t]), pack4(v[2 + 2 * t])), dst + 4096 + t * 1024);
                    STC(join16(pack4(q[1 + 2 * t] * QSCALE), pack4(q[2 + 2 * t] * QSCALE)), WS_Q + ((4 * g + w) * 64 + 2 * i + t) * 1024); }
#pragma unroll
                for (int t = 0; t < 5; ++t) q[t] = k[t] = v[t] = (f32x4){};
                STAMP(1); SBAR();
            }
            if constexpr (s < 14) { TMS(s, cnt[s]); wait_bar<cnt[s]>(); }
            else if constexpr (s == 14) { asm volatile("s_waitcnt lgkmcnt(0)" ::: "memory"); TMS(s, 63); SETW(w, 1); WAITW(4, 1); }
            else { TMS(s, 63); }
            SBAR();
            if constexpr (s < 15) readf(IC<s + 1>{}, fr[(s + 1) & 1]);
            if constexpr (s != 7) mm(S, fr[s & 1]);
            if constexpr (s < 15 && s != 7) {
#pragma unroll
                for (int u = 0; u < 22; ++u) { __builtin_amdgcn_sched_group_barrier(0x8, 1, 0); __builtin_amdgcn_sched_group_barrier(0x100, 1, 0); }
                __builtin_amdgcn_sched_group_barrier(0x8, 8, 0); }
            SBAR();
            if constexpr (s < 4) XLD(s + 4);
            if constexpr (s == 4) tag = mktag(*(LAS const unsigned long long*)(lds + EP_OFF));
            if constexpr (s == PUBS) { VM0(); if (lane == 0) __hip_atomic_store((gu64*)(ws + WS_KVFLAG) + ((4 * g + w) * 32 + i) * FS, tag, RLX_AGENT); }
        });
        BAR();
        __builtin_amdgcn_s_setprio(3);
        STAMP(2);
        u32x2 kf[5], vf[5], of[4];
        f32x4 mc, mp, mp0;
#pragma unroll
        for (int r = 0; r < 4; ++r) { const int dc = l15 - (4 * lg + r); mc[r] = (dc >= 0 && !(dc & 1)) ? 0.f : -3e38f; mp[r] = (dc <= -2 && !(dc & 1)) ? 0.f : -3e38f; mp0[r] = i ? mp[r] : -3e38f; }
#pragma unroll
        for (int t = 0; t < 5; ++t) { kf[t] = pack4(k[t]); vf[t] = pack4(v[t]); }
        const u32x2 one2 = {0x3F803F80u, 0x3F803F80u};
#pragma unroll
        for (int sb = 0; sb < 4; ++sb) {
            const f32x4 z = {}; const u32x2 qs = pack4(q[sb + 1] * QSCALE);
            f32x4 sp = mfma16k16(kf[sb], qs, sb ? mp : mp0), sc = mfma16k16(kf[sb + 1], qs, mc);
            float m = mx(mx(mx(mx(sc[0], sc[1]), sc[2]), mx(mx(sc[3], sp[0]), sp[1])), mx(sp[2], sp[3]));
            m = xmax32(xmax16(m));
#pragma unroll
            for (int r = 0; r < 4; ++r) { sc[r] = __builtin_amdgcn_exp2f(sc[r] - m); sp[r] = __builtin_amdgcn_exp2f(sp[r] - m); }
            const u32x2 pp = pack4(sp), pc = pack4(sc);
            f32x4 l = mfma16k16(one2, pp, z); l = mfma16k16(one2, pc, l);
            f32x4 o = mfma16k16(vf[sb], pp, z); o = mfma16k16(vf[sb + 1], pc, o);
            o *= __builtin_amdgcn_rcpf(l[0]);
            of[sb] = pack4(o);
        }
        STC(join16(of[0], of[1]), WS_OA + (2 * i * 32 + 4 * g + w) * 1024);
        STC(join16(of[2], of[3]), WS_OA + ((2 * i + 1) * 32 + 4 * g + w) * 1024);
        if (KVOLD) { KVLD(2); KVLD(3); }
        STAMP(3);
    }
    STAMP(5);
    const u32x4 qa = lds128(L + SPARE_OFF + wq * 2048 + lane16), qb = lds128(L + SPARE_OFF + wq * 2048 + 1024 + lane16);
    const int tt = w & 1, kq = w >> 1, kh = w >> 2;
    const unsigned osrc = ((2 * i3 + tt) * 32 + 8 * kq) * 1024;
    const unsigned ovoff = (lane >> 3) * 1024 + g3 * 128 + (((lane & 7) ^ (((lane >> 4) & 1) << 2)) * 16);
    u32x4 of[8];
    unsigned long long gg, fb;
    gu64* obf = (gu64*)(ws + WS_OBFLAG) + ((2 * i3 + tt) * 32 + 8 * kq) * FS;
#define POLLB() asm volatile("s_nop 4\n\tglobal_load_dwordx2 %0, %1, %2 sc1" : "=v"(fb) : "v"((lane & 7) * 8 * FS), "s"(obf) : "memory")
    float gv;
    const lds_cptr wo0 = L + kq * 16384 + (8 * (lane >> 5) + ((lane >> 2) & 3)) * 128 + (((lane >> 3) & 1) * 64) + (lg & 1) * 32 + (lane & 3) * 8;
    const lds_cptr wo1 = L + kq * 16384 + (8 * (lane >> 5) + ((lane >> 2) & 3)) * 128 + ((1 ^ ((lane >> 3) & 1)) * 64) + (lg & 1) * 32 + (lane & 3) * 8;
    f32x16 a0 = {}, a1 = {};
    auto proj_a = [&]() {
#pragma unroll
            for (int u = 0; u < 8; ++u) { a0 = mfma32(tr128(wo0 + u * 2048), of[u], a0); a1 = mfma32(tr128(wo1 + u * 2048), of[u], a1); }
            a0 *= gv; a1 *= gv; PIN(a0); PIN(a1); };
    const int sidx = (int)0xDDDDDDDDu;
    const bool real = (lane & 16) == 0;
    auto finish = [&](f32x16 o, const u32x4 qf, int T) {
        float chk = o[8];
#pragma unroll
        for (int r = 0; r < 8; ++r) chk = fmaxf(chk, fabsf(o[r]) * 0x1p-30f);
        if (__builtin_expect(__any(!(o[8] < 0x1p90f && chk < 0x1p90f)), 0)) {
            const f32x16 zero = {}; float m = -3e38f;
            const unsigned ksrc = WS_KV + h * 131072;
#pragma unroll 1
            for (int t = 0; t < 64; ++t) { f32x16 s = mfma32(LDC(ksrc + (t >> 2) * 8192 + (t & 3) * 1024), qf, zero);
#pragma unroll
                for (int r = 0; r < 16; ++r) m = fmaxf(m, s[r]); }
            m = xmax32(m); o = zero;
            const unsigned vl = (lane >> 5) * 512 + l15 * 16;
            const u32x4 ones = {0x3F803F80u, 0x3F803F80u, 0x3F803F80u, 0x3F803F80u};
#pragma unroll 1
            for (int t = 0; t < 64; ++t) { const unsigned ts = ksrc + (t >> 2) * 8192 + (t & 3) * 1024; f32x16 s = mfma32(LDC(ts), qf, zero);
                const u32x4 a = __builtin_amdgcn_raw_buffer_load_b128(wsr, vl, ts + 4096, 16), c = __builtin_amdgcn_raw_buffer_load_b128(wsr, vl + 256, ts + 4096, 16);
#pragma unroll
                for (int r = 0; r < 16; ++r) s[r] = __builtin_amdgcn_exp2f(s[r] - m);
                o = smf(real ? a : ones, lo8(s), o, sidx); o = smf(real ? c : ones, hi8(s), o, sidx); }
        }
        const float rl = __builtin_amdgcn_rcpf(o[8]);
        STC(join32(cvtpk(o[0] * rl, o[1] * rl), cvtpk(o[2] * rl, o[3] * rl), cvtpk(o[4] * rl, o[5] * rl), cvtpk(o[6] * rl, o[7] * rl)), WS_OB + (T * 32 + h) * 1024);
    };
    {
        unsigned kba = lds0 + kh * 8192 + lane16, vba = lds0 + (real ? kh * 8192 + 4096 : ONES_OFF) + (lane >> 5) * 512 + l15 * 16;
        const int vstep = real ? 16384 : 0;
        f32x16 sa, sb, ca, cb, oa = {}, ob = {};
        u32x4 kf = lds128(L + lane16), va = lds128((lds_cptr)(uintptr_t)vba), vc = va;
        { const f32x16 zero = {}; sa = mfma32(kf, qa, zero); sb = mfma32(kf, qb, zero); kf = lds128((lds_cptr)(uintptr_t)kba);
          float ma = sa[0], mb = sb[0];
#pragma unroll
          for (int r = 1; r < 16; ++r) { ma = mx(ma, sa[r]); mb = mx(mb, sb[r]); }
          ma = xmax32(ma); mb = xmax32(mb);
#pragma unroll
          for (int r = 0; r < 16; ++r) { ca[r] = -ma; cb[r] = -mb; sb[r] = 0.f; } }
        PIN(ca); PIN(cb);
        sa = mfma32(kf, qa, ca);
        auto run = [&](int n) {
            asm volatile("s_waitcnt lgkmcnt(0)\n\ts_nop 11\n"
            "1:\n\t"
            "v_exp_f32 v32, v32\n\tv_exp_f32 v33, v33\n\tv_smfmac_f32_32x32x32_bf16 %[ob], %[vc], v[56:63], %[ix]\n\tds_read_b128 %[vc], %[vb] offset:256\n\tv_exp_f32 v34, v34\n\tv_exp_f32 v35, v35\n\tv_exp_f32 v36, v36\n\tv_exp_f32 v37, v37\n\tv_mfma_f32_32x32x16_bf16 v[48:63], %[kf], %[qb], %[cb]\n\tds_read_b128 %[kf], %[kb] offset:1024\n\tv_exp_f32 v38, v38\n\tv_exp_f32 v39, v39\n\tv_exp_f32 v40, v40\n\tv_exp_f32 v41, v41\n\ts_waitcnt lgkmcnt(2)\n\tv_smfmac_f32_32x32x32_bf16 %[oa], %[va], v[32:39], %[ix]\n\tv_exp_f32 v42, v42\n\tv_exp_f32 v43, v43\n\tv_exp_f32 v44, v44\n\tv_exp_f32 v45, v45\n\tv_exp_f32 v46, v46\n\tv_exp_f32 v47, v47\n\t"
            "v_exp_f32 v48, v48\n\tv_exp_f32 v49, v49\n\ts_waitcnt lgkmcnt(0)\n\tv_smfmac_f32_32x32x32_bf16 %[oa], %[vc], v[40:47], %[ix]\n\tv_exp_f32 v50, v50\n\tv_exp_f32 v51, v51\n\tv_exp_f32 v52, v52\n\tv_exp_f32 v53, v53\n\tv_mfma_f32_32x32x16_bf16 v[32:47], %[kf], %[qa], %[ca]\n\tv_exp_f32 v54, v54\n\tv_exp_f32 v55, v55\n\tv_exp_f32 v56, v56\n\tv_exp_f32 v57, v57\n\tv_smfmac_f32_32x32x32_bf16 %[ob], %[va], v[48:55], %[ix]\n\tds_read_b128 %[va], %[vb] offset:1024\n\tv_exp_f32 v58, v58\n\tv_exp_f32 v59, v59\n\tv_exp_f32 v60, v60\n\tv_exp_f32 v61, v61\n\tv_exp_f32 v62, v62\n\tv_exp_f32 v63, v63\n\t"
            "v_exp_f32 v32, v32\n\tv_exp_f32 v33, v33\n\tv_smfmac_f32_32x32x32_bf16 %[ob], %[vc], v[56:63], %[ix]\n\tds_read_b128 %[vc], %[vb] offset:1280\n\tv_exp_f32 v34, v34\n\tv_exp_f32 v35, v35\n\tv_exp_f32 v36, v36\n\tv_exp_f32 v37, v37\n\tv_mfma_f32_32x32x16_bf16 v[48:63], %[kf], %[qb], %[cb]\n\tds_read_b128 %[kf], %[kb] offset:2048\n\tv_exp_f32 v38, v38\n\tv_exp_f32 v39, v39\n\tv_exp_f32 v40, v40\n\tv_exp_f32 v41, v41\n\ts_waitcnt lgkmcnt(2)\n\tv_smfmac_f32_32x32x32_bf16 %[oa], %[va], v[32:39], %[ix]\n\tv_exp_f32 v42, v42\n\tv_exp_f32 v43, v43\n\tv_exp_f32 v44, v44\n\tv_exp_f32 v45, v45\n\tv_exp_f32 v46, v46\n\tv_exp_f32 v47, v47\n\t"
            "v_exp_f32 v48, v48\n\tv_exp_f32 v49, v49\n\ts_waitcnt lgkmcnt(0)\n\tv_smfmac_f32_32x32x32_bf16 %[oa], %[vc], v[40:47], %[ix]\n\tv_exp_f32 v50, v50\n\tv_exp_f32 v51, v51\n\tv_exp_f32 v52, v52\n\tv_exp_f32 v53, v53\n\tv_mfma_f32_32x32x16_bf16 v[32:47], %[kf], %[qa], %[ca]\n\tv_exp_f32 v54, v54\n\tv_exp_f32 v55, v55\n\tv_exp_f32 v56, v56\n\tv_exp_f32 v57, v57\n\tv_smfmac_f32_32x32x32_bf16 %[ob], %[va], v[48:55], %[ix]\n\tds_read_b128 %[va], %[vb] offset:2048\n\tv_exp_f32 v58, v58\n\tv_exp_f32 v59, v59\n\tv_exp_f32 v60, v60\n\tv_exp_f32 v61, v61\n\tv_exp_f32 v62, v62\n\tv_exp_f32 v63, v63\n\t"
            "v_exp_f32 v32, v32\n\tv_exp_f32 v33, v33\n\tv_smfmac_f32_32x32x32_bf16 %[ob], %[vc], v[56:63], %[ix]\n\tds_read_b128 %[vc], %[vb] offset:2304\n\tv_exp_f32 v34, v34\n\tv_exp_f32 v35, v35\n\tv_exp_f32 v36, v36\n\tv_exp_f32 v37, v37\n\tv_mfma_f32_32x32x16_bf16 v[48:63], %[kf], %[qb], %[cb]\n\tds_read_b128 %[kf], %[kb] offset:3072\n\tv_exp_f32 v38, v38\n\tv_exp_f32 v39, v39\n\tv_exp_f32 v40, v40\n\tv_exp_f32 v41, v41\n\ts_waitcnt lgkmcnt(2)\n\tv_smfmac_f32_32x32x32_bf16 %[oa], %[va], v[32:39], %[ix]\n\tv_exp_f32 v42, v42\n\tv_exp_f32 v43, v43\n\tv_exp_f32 v44, v44\n\tv_exp_f32 v45, v45\n\tv_exp_f32 v46, v46\n\tv_exp_f32 v47, v47\n\t"
            "v_exp_f32 v48, v48\n\tv_exp_f32 v49, v49\n\ts_waitcnt lgkmcnt(0)\n\tv_smfmac_f32_32x32x32_bf16 %[oa], %[vc], v[40:47], %[ix]\n\tv_exp_f32 v50, v50\n\tv_exp_f32 v51, v51\n\tv_exp_f32 v52, v52\n\tv_exp_f32 v53, v53\n\tv_mfma_f32_32x32x16_bf16 v[32:47], %[kf], %[qa], %[ca]\n\tv_exp_f32 v54, v54\n\tv_exp_f32 v55, v55\n\tv_exp_f32 v56, v56\n\tv_exp_f32 v57, v57\n\tv_smfmac_f32_32x32x32_bf16 %[ob], %[va], v[48:55], %[ix]\n\tds_read_b128 %[va], %[vb] offset:3072\n\tv_exp_f32 v58, v58\n\tv_exp_f32 v59, v59\n\tv_exp_f32 v60, v60\n\tv_exp_f32 v61, v61\n\tv_exp_f32 v62, v62\n\tv_exp_f32 v63, v63\n\t"
            "v_exp_f32 v32, v32\n\tv_exp_f32 v33, v33\n\tv_smfmac_f32_32x32x32_bf16 %[ob], %[vc], v[56:63], %[ix]\n\tds_read_b128 %[vc], %[vb] offset:3328\n\tv_exp_f32 v34, v34\n\tv_exp_f32 v35, v35\n\tv_exp_f32 v36, v36\n\tv_exp_f32 v37, v37\n\tv_mfma_f32_32x32x16_bf16 v[48:63], %[kf], %[qb], %[cb]\n\tds_read_b128 %[kf], %[kb] offset:16384\n\tv_exp_f32 v38, v38\n\tv_exp_f32 v39, v39\n\tv_exp_f32 v40, v40\n\tv_exp_f32 v41, v41\n\ts_waitcnt lgkmcnt(2)\n\tv_smfmac_f32_32x32x32_bf16 %[oa], %[va], v[32:39], %[ix]\n\tv_exp_f32 v42, v42\n\tv_exp_f32 v43, v43\n\tv_exp_f32 v44, v44\n\tv_exp_f32 v45, v45\n\tv_exp_f32 v46, v46\n\tv_exp_f32 v47, v47\n\t"
            "v_exp_f32 v48, v48\n\tv_exp_f32 v49, v49\n\ts_waitcnt lgkmcnt(0)\n\tv_smfmac_f32_32x32x32_bf16 %[oa], %[vc], v[40:47], %[ix]\n\tv_exp_f32 v50, v50\n\tv_exp_f32 v51, v51\n\tv_exp_f32 v52, v52\n\tv_exp_f32 v53, v53\n\tv_mfma_f32_32x32x16_bf16 v[32:47], %[kf], %[qa], %[ca]\n\tv_exp_f32 v54, v54\n\tv_exp_f32 v55, v55\n\tv_exp_f32 v56, v56\n\tv_exp_f32 v57, v57\n\tv_add_u32 %[vb], %[vb], %[vs]\n\tv_smfmac_f32_32x32x32_bf16 %[ob], %[va], v[48:55], %[ix]\n\tds_read_b128 %[va], %[vb] offset:0\n\tv_add_u32 %[kb], 0x4000, %[kb]\n\ts_sub_u32 %[n], %[n], 1\n\tv_exp_f32 v58, v58\n\tv_exp_f32 v59, v59\n\tv_exp_f32 v60, v60\n\tv_exp_f32 v61, v61\n\tv_exp_f32 v62, v62\n\tv_exp_f32 v63, v63\n\t"
            "s_cmp_lg_u32 %[n], 0\n\ts_cbranch_scc1 1b\n\ts_waitcnt lgkmcnt(0)\n\ts_nop 7"
            : "+{v[32:47]}"(sa), "+{v[48:63]}"(sb), [oa] "+v"(oa), [ob] "+v"(ob), [kf] "+v"(kf), [va] "+v"(va), [vc] "+v"(vc), [kb] "+v"(kba), [vb] "+v"(vba), [n] "+s"(n)
            : [qa] "v"(qa), [qb] "v"(qb), [ca] "v"(ca), [cb] "v"(cb), [vs] "v"(vstep), [ix] "v"(sidx) : "scc", "memory");
        };
        STAMP(12);
        run(2);
        __builtin_amdgcn_s_setprio(2);
        run(2);
        STAMP(13);
        if (w < 4) { VM0(); if (lane == 0) __hip_atomic_store((gu64*)(ws + WS_OAFLAG) + (i * 32 + 4 * g + w) * FS, tag, RLX_AGENT); }
        SETW(8 + w, 1);
        WAITW(8, 1);
        kf = lds128((lds_cptr)(uintptr_t)kba); va = lds128((lds_cptr)(uintptr_t)vba); sa = mfma32(kf, qa, ca);
        if (w >= 4) WAITW(12, 1);
#pragma unroll
        for (int u = 0; u < 8; ++u) glds1(woa + (size_t)(64 * (2 * wq + kh) + 8 * u) * 1024, ovoff, lds0 + (2 * wq + kh) * 8192 + u * 1024);
        if (w < 2) touch(wob + g3 * 128, (128 * (idx & 3) + 64 * w + lane) * 1024, lds0 + GATE_OFF + 512);
        __builtin_amdgcn_s_setprio(1);
        STAMP(14);
        run(SEG3);
        STAMP(15);
#pragma unroll 1
        for (unsigned spins = 0; spins < kSpinCap; ++spins) {
            const unsigned long long f = __hip_atomic_load((gu64*)(ws + WS_OAFLAG) + (i3 * 32 + 8 * kq + (lane & 7)) * FS, RLX_AGENT);
            gg = __hip_atomic_load((gu64*)(ws + WS_GATE) + 64 * i3 + 32 * tt + (lane & 31), RLX_AGENT);
            if (__all(f == tag && (gg >> 32) == (tag >> 32))) break;
            __builtin_amdgcn_s_sleep(1); }
        __builtin_amdgcn_fence(__ATOMIC_ACQUIRE, "wavefront");
#pragma unroll
        for (int u = 0; u < 8; ++u) of[u] = LDC(WS_OA + osrc + u * 1024);
        SETW(8 + w, 2);
        __builtin_amdgcn_s_setprio(0);
        STAMP(8);
        run(4 - SEG3);
#pragma unroll
        for (int u = 0; u < 8; ++u) PIN(of[u]);
        if (w >= 4) POLLB();
        ob = smf(vc, hi8(sb), ob, sidx);
        STAMP(6);
        f32x16 give, o;
#pragma unroll
        for (int r = 0; r < 16; ++r) { give[r] = r < 9 ? (kh ? oa[r] : ob[r]) : 0.f; o[r] = r < 9 ? (kh ? ob[r] : oa[r]) : 0.f; }
        LAS char* xw = (LAS char*)lds + SPARE_OFF + w * 3072 + lane16;
        *(LAS f32x4*)xw = __builtin_shufflevector(give, give, 0, 1, 2, 3); *(LAS f32x4*)(xw + 1024) = __builtin_shufflevector(give, give, 4, 5, 6, 7); *(LAS float*)(xw + 2048) = give[8];
        gv = __uint_as_float((unsigned)gg);
        if (w < 4) {
            WAITW(8, 2); WAITW(12, 2);
            proj_a();
            SETW(8 + w, 3); WAITW(8, 3);
#pragma unroll
            for (int u = 0; u < 16; ++u) glds1(wob + (size_t)(128 * w + 8 * u) * 1024, ovoff, lds0 + (w < 2 ? w * 16384 + u * 1024 : w * 32768 + (u >> 3) * 16384 + (u & 7) * 1024));
            VM0(); POLLB(); }
        STAMP(9);
        BAR();
        STAMP(23);
        LAS const char* xr = L + SPARE_OFF + (w ^ 4) * 3072 + lane16;
        const f32x4 r0 = *(LAS const f32x4*)xr, r1 = *(LAS const f32x4*)(xr + 1024);
#pragma unroll
        for (int r = 0; r < 4; ++r) { o[r] += r0[r]; o[4 + r] += r1[r]; }
        o[8] += *(LAS const float*)(xr + 2048);
        finish(o, kh ? qb : qa, 8 * j + 2 * wq + kh);
        STAMP(7);
    }
    {
        f32x16 b0 = {}, b1 = {};
        u32x4 wf[16], ob8[8];
        asm volatile("s_waitcnt vmcnt(1)" : "+v"(fb) :: "memory");
        unsigned got = (unsigned)__builtin_amdgcn_ballot_w64(fb == tag) & 0xffu;
        const char* pb = ws + WS_OB + osrc + 4096; unsigned long long st;
        asm volatile("s_nop 4\n\t"
            "s_bitcmp1_b32 %[got], 0\n\ts_cselect_b64 %[t], %[pb], %[pa]\n\tglobal_load_dwordx4 %0, %[vo], %[t] offset:-4096 sc1\n\t"
            "s_bitcmp1_b32 %[got], 1\n\ts_cselect_b64 %[t], %[pb], %[pa]\n\tglobal_load_dwordx4 %1, %[vo], %[t] offset:-3072 sc1\n\t"
            "s_bitcmp1_b32 %[got], 2\n\ts_cselect_b64 %[t], %[pb], %[pa]\n\tglobal_load_dwordx4 %2, %[vo], %[t] offset:-2048 sc1\n\t"
            "s_bitcmp1_b32 %[got], 3\n\ts_cselect_b64 %[t], %[pb], %[pa]\n\tglobal_load_dwordx4 %3, %[vo], %[t] offset:-1024 sc1\n\t"
            "s_bitcmp1_b32 %[got], 4\n\ts_cselect_b64 %[t], %[pb], %[pa]\n\tglobal_load_dwordx4 %4, %[vo], %[t] offset:0 sc1\n\t"
            "s_bitcmp1_b32 %[got], 5\n\ts_cselect_b64 %[t], %[pb], %[pa]\n\tglobal_load_dwordx4 %5, %[vo], %[t] offset:1024 sc1\n\t"
            "s_bitcmp1_b32 %[got], 6\n\ts_cselect_b64 %[t], %[pb], %[pa]\n\tglobal_load_dwordx4 %6, %[vo], %[t] offset:2048 sc1\n\t"
            "s_bitcmp1_b32 %[got], 7\n\ts_cselect_b64 %[t], %[pb], %[pa]\n\tglobal_load_dwordx4 %7, %[vo], %[t] offset:3072 sc1\n\t"
            : "=&v"(ob8[0]), "=&v"(ob8[1]), "=&v"(ob8[2]), "=&v"(ob8[3]), "=&v"(ob8[4]), "=&v"(ob8[5]), "=&v"(ob8[6]), "=&v"(ob8[7]), [t] "=&s"(st)
            : [got] "s"(got), [pb] "s"(pb), [pa] "s"(ws + WS_OA + osrc + 4096), [vo] "v"(lane16) : "scc", "memory");
        asm volatile("s_waitcnt vmcnt(8)" ::: "memory");
        if (lane == 0) __hip_atomic_store((gu64*)(ws + WS_OBFLAG) + ((8 * j + 2 * wq + kh) * 32 + h) * FS, tag, RLX_AGENT);
        STAMP(22);
        if (w >= 4) { proj_a(); SETW(8 + w, 3); }
        const int wb0 = kq < 2 ? 0 : kq * 16384, wb1 = kq < 2 ? 0 : kq * 16384 + 8192;
#pragma unroll
        for (int u = 0; u < 8; ++u) { wf[2 * u] = tr128(wo0 + (u < 4 ? wb0 : wb1) + u * 2048); wf[2 * u + 1] = tr128(wo1 + (u < 4 ? wb0 : wb1) + u * 2048); }
        STAMP(21);
        { unsigned fresh, spins = kSpinCap;
          asm volatile("s_nop 4\n"
            "2:\n\ts_cmp_eq_u32 %[got], 0xff\n\ts_cbranch_scc1 3f\n\t"
            "global_load_dwordx2 %[fb], %[fo], %[fp] sc1\n\ts_waitcnt vmcnt(0)\n\t"
            "v_cmp_eq_u64 vcc, %[tg], %[fb]\n\ts_nop 1\n\ts_and_b32 %[fr], vcc_lo, 0xff\n\ts_andn2_b32 %[fr], %[fr], %[got]\n\t"
            "s_bitcmp1_b32 %[fr], 0\n\ts_cbranch_scc0 10f\n\tglobal_load_dwordx4 %0, %[vo], %[pb] offset:-4096 sc1\n10:\n\t"
            "s_bitcmp1_b32 %[fr], 1\n\ts_cbranch_scc0 11f\n\tglobal_load_dwordx4 %1, %[vo], %[pb] offset:-3072 sc1\n11:\n\t"
            "s_bitcmp1_b32 %[fr], 2\n\ts_cbranch_scc0 12f\n\tglobal_load_dwordx4 %2, %[vo], %[pb] offset:-2048 sc1\n12:\n\t"
            "s_bitcmp1_b32 %[fr], 3\n\ts_cbranch_scc0 13f\n\tglobal_load_dwordx4 %3, %[vo], %[pb] offset:-1024 sc1\n13:\n\t"
            "s_bitcmp1_b32 %[fr], 4\n\ts_cbranch_scc0 14f\n\tglobal_load_dwordx4 %4, %[vo], %[pb] offset:0 sc1\n14:\n\t"
            "s_bitcmp1_b32 %[fr], 5\n\ts_cbranch_scc0 15f\n\tglobal_load_dwordx4 %5, %[vo], %[pb] offset:1024 sc1\n15:\n\t"
            "s_bitcmp1_b32 %[fr], 6\n\ts_cbranch_scc0 16f\n\tglobal_load_dwordx4 %6, %[vo], %[pb] offset:2048 sc1\n16:\n\t"
            "s_bitcmp1_b32 %[fr], 7\n\ts_cbranch_scc0 17f\n\tglobal_load_dwordx4 %7, %[vo], %[pb] offset:3072 sc1\n17:\n\t"
            "s_or_b32 %[got], %[got], %[fr]\n\ts_sleep 1\n\ts_sub_u32 %[sp], %[sp], 1\n\ts_cbranch_scc0 2b\n"
            "3:\n\ts_waitcnt vmcnt(0)"
            : "+v"(ob8[0]), "+v"(ob8[1]), "+v"(ob8[2]), "+v"(ob8[3]), "+v"(ob8[4]), "+v"(ob8[5]), "+v"(ob8[6]), "+v"(ob8[7]), [got] "+s"(got), [fr] "=&s"(fresh), [sp] "+s"(spins), [fb] "=&v"(fb)
            : [pb] "s"(pb), [vo] "v"(lane16), [fo] "v"((lane & 7) * 8 * FS), [fp] "s"(obf), [tg] "s"(tag) : "scc", "vcc", "memory"); }
        STAMP(16);
        STAMP(10);
#pragma unroll
        for (int u = 0; u < 8; ++u) { b0 = mfma32(wf[2 * u], ob8[u], b0); b1 = mfma32(wf[2 * u + 1], ob8[u], b1); }
        a0 += b0 * (1.f - gv); a1 += b1 * (1.f - gv);
        STAMP(18);
        WAITW(12, 3);
        const int cs = tt ? 1024 : 512;
        LAS f32x4* rw = (LAS f32x4*)(lds + (tt ? 73728 : 32768) + kq * 2048 + lane16);
        rw[0] = __builtin_shufflevector(a0, a0, 0, 1, 2, 3); rw[64] = __builtin_shufflevector(a0, a0, 4, 5, 6, 7);
        rw[cs] = __builtin_shufflevector(a0, a0, 8, 9, 10, 11); rw[cs + 64] = __builtin_shufflevector(a0, a0, 12, 13, 14, 15);
        rw[2 * cs] = __builtin_shufflevector(a1, a1, 0, 1, 2, 3); rw[2 * cs + 64] = __builtin_shufflevector(a1, a1, 4, 5, 6, 7);
        rw[3 * cs] = __builtin_shufflevector(a1, a1, 8, 9, 10, 11); rw[3 * cs + 64] = __builtin_shufflevector(a1, a1, 12, 13, 14, 15);
        STAMP(19);
        BAR();
        STAMP(20);
        if (blockIdx.x == 0 && tid == 0) __hip_atomic_store((gu64*)(ws + WS_EPOCH), tag, RLX_AGENT);
        LAS const f32x4* rr = (LAS const f32x4*)(lds + (tt ? 73728 : 32768) + kq * cs * 16 + lane16);
        const f32x4 r0 = (rr[0] + rr[128]) + (rr[256] + rr[384]), r1 = (rr[64] + rr[192]) + (rr[320] + rr[448]);
        *(u32x4*)(out + (size_t)(64 * i3 + 32 * tt + (lane & 31)) * 1024 + g3 * 128 + kq * 32 + (lane >> 5) * 16) =
            join32(cvtpk(r0[0], r0[1]), cvtpk(r0[2], r0[3]), cvtpk(r1[0], r1[1]), cvtpk(r1[2], r1[3]));
    }
    STAMP(11);
    STAMP_DUMP();
}
}

extern "C" void kernel_launch(void* const* d_in, const int* in_sizes, int n_in, void* d_out, int out_size, void* d_ws, size_t ws_size, hipStream_t stream) {
    fused_kernel<<<256, 512, 0, stream>>>((const char*)d_in[0], (const char*)d_in[1], (const char*)d_in[2], (const char*)d_in[4], (const char*)d_in[5],
                                          (const char*)d_in[6], (char*)d_out, (char*)d_ws);
}
```

```cpp
#include <hip/hip_runtime.h>
#include <hip/hip_bf16.h>
#include <stdint.h>

#define STAMPS 0

#define GAS __attribute__((address_space(1)))
#define LAS __attribute__((address_space(3)))
#define RLX_AGENT __ATOMIC_RELAXED, __HIP_MEMORY_SCOPE_AGENT
typedef unsigned u32x2 __attribute__((ext_vector_type(2)));
typedef unsigned u32x4 __attribute__((ext_vector_type(4)));
typedef float f32x2 __attribute__((ext_vector_type(2)));
typedef float f32x4 __attribute__((ext_vector_type(4)));
typedef float f32x8 __attribute__((ext_vector_type(8)));
typedef float f32x16 __attribute__((ext_vector_type(16)));
typedef __bf16 bf16x2 __attribute__((ext_vector_type(2)));
typedef __bf16 bf16x8 __attribute__((ext_vector_type(8)));
typedef __bf16 bf16x16 __attribute__((ext_vector_type(16)));
typedef short s16x4 __attribute__((ext_vector_type(4)));
typedef GAS unsigned long long gu64;
typedef LAS const char* lds_cptr;

#define SEG3 1
#define GKS 2
#define GVS 4
#define LAB 1
#define TORD 1
#define HSH 4
#define PSL 1
#define ONEPOLL 0
#define FSTRIDE 8
namespace {
constexpr unsigned WS_EPOCH = 0;
constexpr int FS = FSTRIDE;
constexpr unsigned WS_KVFLAG = 11u << 20;
constexpr unsigned WS_OAFLAG = WS_KVFLAG + 1024 * FS * 8;
constexpr unsigned WS_OBFLAG = WS_OAFLAG + 1024 * FS * 8;
constexpr unsigned WS_GATE = 294912;
constexpr unsigned WS_STAMP = 32768;
constexpr unsigned WS_Q = 1u << 20;
constexpr unsigned WS_KV = WS_Q + (2u << 20);
constexpr unsigned WS_OA = WS_KV + (4u << 20);
constexpr unsigned WS_OB = WS_OA + (2u << 20);
constexpr unsigned WS_END = WS_OB + (2u << 20);
static_assert(WS_END <= WS_KVFLAG, "ws");
constexpr int X_OFF = 0, XKS = 80 * 128, WR_OFF = 8 * XKS, WSLOT = 24576;
constexpr int GW_OFF = WR_OFF + 3 * WSLOT, GATE_OFF = GW_OFF + 1024, ONES_OFF = 159744, LDS_BYTES = 163840;
constexpr int PROG_OFF = GATE_OFF + 256, EP_OFF = GATE_OFF;
constexpr int SPARE_OFF = 131072;
static_assert(GATE_OFF + 512 <= ONES_OFF && SPARE_OFF + 8 * 3072 <= GW_OFF, "lds");
constexpr float QSCALE = 0.25f * 1.4426950408889634f;
constexpr unsigned kSpinCap = 1u << 18;

#define SBAR() __builtin_amdgcn_sched_barrier(0)
#define PIN(x) asm volatile("" : "+v"(x))

__device__ __forceinline__ unsigned cvtpk(float lo, float hi) { return __builtin_bit_cast(unsigned, __builtin_convertvector((f32x2){lo, hi}, bf16x2)); }
#define GLDS_OPEN "s_mov_b32 %0, m0\n\ts_mov_b32 m0, %3\n\ts_nop 2\n\t"
#define GLDS_ARGS : "=&s"(sv) : "v"(voff), "s"(sbase), "s"(ldsb) : "memory"
__device__ __forceinline__ void glds1(const void* sbase, unsigned voff, unsigned ldsb) { unsigned sv; asm volatile(GLDS_OPEN "global_load_lds_dwordx4 %1, %2\n\ts_mov_b32 m0, %0" GLDS_ARGS); }
__device__ __forceinline__ void glds3(const void* sbase, unsigned voff, unsigned v1, unsigned v2, unsigned ldsb) { unsigned sv;
    asm volatile(GLDS_OPEN "global_load_lds_dwordx4 %1, %2\n\tglobal_load_lds_dwordx4 %4, %2 offset:1024\n\tglobal_load_lds_dwordx4 %5, %2 offset:2048\n\ts_mov_b32 m0, %0"
                 : "=&s"(sv) : "v"(voff), "s"(sbase), "s"(ldsb), "v"(v1), "v"(v2) : "memory"); }
__device__ __forceinline__ void glds4c(const void* sbase, unsigned voff, unsigned ldsb) { unsigned sv;
    asm volatile(GLDS_OPEN "global_load_lds_dwordx4 %1, %2 sc1\n\tglobal_load_lds_dwordx4 %1, %2 offset:1024 sc1\n\tglobal_load_lds_dwordx4 %1, %2 offset:2048 sc1\n\tglobal_load_lds_dwordx4 %1, %2 offset:3072 sc1\n\ts_mov_b32 m0, %0" GLDS_ARGS); }
__device__ __forceinline__ void touch(const void* sbase, unsigned voff, unsigned ldsb) { unsigned sv; asm volatile(GLDS_OPEN "global_load_lds_dword %1, %2\n\ts_mov_b32 m0, %0" GLDS_ARGS); }
__device__ __forceinline__ void glds2c(const void* sbase, unsigned voff, unsigned ldsb) { unsigned sv;
    asm volatile(GLDS_OPEN "global_load_lds_dwordx4 %1, %2 sc1\n\tglobal_load_lds_dwordx4 %1, %2 offset:1024 sc1\n\ts_mov_b32 m0, %0" GLDS_ARGS); }
__device__ __forceinline__ u32x2 tr64(lds_cptr p) { return __builtin_bit_cast(u32x2, __builtin_amdgcn_ds_read_tr16_b64_v4i16((LAS s16x4*)p)); }
__device__ __forceinline__ u32x4 tr128(lds_cptr p) { const u32x2 a = tr64(p), b = tr64(p + 512); return (u32x4){a[0], a[1], b[0], b[1]}; }
__device__ __forceinline__ u32x4 lds128(lds_cptr p) { return *(LAS const u32x4*)p; }
__device__ __forceinline__ f32x4 mfma16(u32x4 a, u32x4 b, f32x4 c) { return __builtin_amdgcn_mfma_f32_16x16x32_bf16(__builtin_bit_cast(bf16x8, a), __builtin_bit_cast(bf16x8, b), c, 0, 0, 0); }
__device__ __forceinline__ f32x4 mfma16k16(u32x2 a, u32x2 b, f32x4 c) { return __builtin_amdgcn_mfma_f32_16x16x16bf16_1k(__builtin_bit_cast(s16x4, a), __builtin_bit_cast(s16x4, b), c, 0, 0, 0); }
__device__ __forceinline__ f32x16 mfma32(u32x4 a, u32x4 b, f32x16 c) { return __builtin_amdgcn_mfma_f32_32x32x16_bf16(__builtin_bit_cast(bf16x8, a), __builtin_bit_cast(bf16x8, b), c, 0, 0, 0); }
__device__ __forceinline__ f32x16 smf(u32x4 a, f32x8 b, f32x16 c, int idx) { return __builtin_amdgcn_smfmac_f32_32x32x32_bf16(__builtin_bit_cast(bf16x8, a), __builtin_bit_cast(bf16x16, b), c, idx, 0, 0); }
__device__ __forceinline__ u32x2 pack4(f32x4 v) { return (u32x2){cvtpk(v[0], v[1]), cvtpk(v[2], v[3])}; }
__device__ __forceinline__ u32x4 join16(u32x2 x, u32x2 y) {
    auto a = __builtin_amdgcn_permlane16_swap(x[0], y[0], false, false); auto b = __builtin_amdgcn_permlane16_swap(x[1], y[1], false, false);
    return (u32x4){a[0], b[0], a[1], b[1]}; }
__device__ __forceinline__ u32x4 join32(unsigned a0, unsigned a1, unsigned b0, unsigned b1) {
    auto p0 = __builtin_amdgcn_permlane32_swap(a0, b0, false, false); auto p1 = __builtin_amdgcn_permlane32_swap(a1, b1, false, false);
    return (u32x4){p0[0], p1[0], p0[1], p1[1]}; }
__device__ __forceinline__ float mx(float a, float b) { return __builtin_elementwise_maximum(a, b); }
__device__ __forceinline__ float xmax16(float v) { auto r = __builtin_amdgcn_permlane16_swap(__float_as_uint(v), __float_as_uint(v), false, false); return mx(__uint_as_float(r[0]), __uint_as_float(r[1])); }
__device__ __forceinline__ float xmax32(float v) { auto r = __builtin_amdgcn_permlane32_swap(__float_as_uint(v), __float_as_uint(v), false, false); return mx(__uint_as_float(r[0]), __uint_as_float(r[1])); }
__device__ __forceinline__ float xadd16(float v) { auto r = __builtin_amdgcn_permlane16_swap(__float_as_uint(v), __float_as_uint(v), false, false); return __uint_as_float(r[0]) + __uint_as_float(r[1]); }
__device__ __forceinline__ float xadd32(float v) { auto r = __builtin_amdgcn_permlane32_swap(__float_as_uint(v), __float_as_uint(v), false, false); return __uint_as_float(r[0]) + __uint_as_float(r[1]); }
__device__ __forceinline__ f32x8 lo8(const f32x16& v) { return __builtin_shufflevector(v, v, 0, 1, 2, 3, 4, 5, 6, 7); }
__device__ __forceinline__ f32x8 hi8(const f32x16& v) { return __builtin_shufflevector(v, v, 8, 9, 10, 11, 12, 13, 14, 15); }

__device__ __forceinline__ void wait_flags(gu64* f, int mask, int lane, unsigned long long tag) {
    gu64* p = f + (lane & mask) * FS;
#pragma unroll 1
    for (unsigned spins = 0; spins < kSpinCap; ++spins) {
        const unsigned long long v = __hip_atomic_load(p, RLX_AGENT);
        if (__all(v == tag)) break;
        __builtin_amdgcn_s_sleep(PSL);
    }
    __builtin_amdgcn_fence(__ATOMIC_ACQUIRE, "wavefront");
}

#define STAMP(k) do {} while (0)
#define TMS(s, c) do {} while (0)
#define STAMP_DUMP() do {} while (0)

template <int N> struct IC { static constexpr int v = N; };
template <int B, int E, class F> __device__ __forceinline__ void static_for(F&& f) { if constexpr (B < E) { f(IC<B>{}); static_for<B + 1, E>(f); } }
template <int N> __device__ __forceinline__ void wait_bar() { asm volatile("s_waitcnt vmcnt(%0) lgkmcnt(0)\n\ts_barrier" :: "i"(N) : "memory"); }
struct Frags { u32x4 x[2][5], w[2][3]; };

__global__ __launch_bounds__(512, 2) void fused_kernel(const char* __restrict__ x, const char* __restrict__ wqa, const char* __restrict__ woa,
                                                       const char* __restrict__ wqb, const char* __restrict__ wob, const char* __restrict__ gw,
                                                       char* __restrict__ out, char* ws) {
    __shared__ __attribute__((aligned(256))) char lds[LDS_BYTES];
    const int tid = threadIdx.x, lane = tid & 63, w = __builtin_amdgcn_readfirstlane(tid >> 6);
    const int xcd = blockIdx.x & 7, idx = blockIdx.x >> 3;
    const int g = 2 * (xcd >> 1) + (idx & 1), i = 16 * (xcd & 1) + (idx >> 1);
    const int h = 4 * ((xcd + HSH) & 7) + (idx & 3), j = idx >> 2;
    const int i3 = 4 * xcd + (idx & 3), g3 = idx >> 2;
    const unsigned lds0 = (unsigned)(uintptr_t)(LAS char*)lds;
    const lds_cptr L = (lds_cptr)lds;
    const __amdgpu_buffer_rsrc_t wsr = __builtin_amdgcn_make_buffer_rsrc(ws, 0, WS_END, 0x00020000);
    const int lane16 = lane * 16, l15 = lane & 15, lg = lane >> 4, wq = w & 3;
#define LDC(soff) __builtin_amdgcn_raw_buffer_load_b128(wsr, lane16, (soff), 16)
#define STC(v, soff) __builtin_amdgcn_raw_buffer_store_b128((v), wsr, lane16 + (soff), 0, 16)
#define BAR() asm volatile("s_waitcnt lgkmcnt(0)\n\ts_barrier" ::: "memory")
#define VM0() asm volatile("s_waitcnt vmcnt(0)" ::: "memory")
    STAMP(0);
    const int rot = (i + 4 * (g & 1) + (g >> 1)) & 7;
#define KS(s) (((s) + rot) & 7)
    auto mktag = [&](unsigned long long ev) { const unsigned long long ep = ((unsigned long long)__builtin_amdgcn_readfirstlane((unsigned)(ev >> 32)) << 32) | (unsigned)__builtin_amdgcn_readfirstlane((unsigned)ev);
        unsigned long long z = (ep + 0x9E3779B97F4A7C15ull) * 0xBF58476D1CE4E5B9ull; z ^= z >> 29; return ((z ^ ep) >> 32) ? z : ~z; };
    unsigned long long tag;
#define TCHB 0
#define TCH 1
#define EPOLL 0
#define KVOLD 1
#define SPF -1
#define LPRIO 3
#define PUBS 9
#define PFKB 21
#define CODE_PREFETCH() do { if (PFKB) { const char* sbase = (const char*)__builtin_amdgcn_s_getpc(); sbase -= (uintptr_t)sbase & 127; const unsigned voff = ((idx * 8 + w) * 4 + (lane & 3)) % (PFKB * 8) * 128, ldsb = lds0 + GATE_OFF + 512; unsigned sv; \
        asm volatile(GLDS_OPEN "global_load_lds_dword %1, %2\n\ts_mov_b32 m0, %0" GLDS_ARGS); } } while (0)
    const int xsw = (l15 >> 1) & 7;
    const lds_cptr xrd0 = L + X_OFF + l15 * 128 + ((lg ^ xsw) * 16), xrd1 = L + X_OFF + l15 * 128 + (((lg + 4) ^ xsw) * 16);
    const unsigned xv0 = (lane >> 3) * 1024 + (((lane & 7) ^ lg) * 16), xv1 = (xv0 ^ 64) + 8192;
    const int gk = (g + GKS) & 7, gv_ = (g + GVS) & 7;
    const unsigned wvl = (lane >> 3) * 3072 + (((lane & 7) ^ ((lg & 1) << 1)) * 16);
    auto first = [&](int s) { const unsigned v_ = wvl ^ ((w & 1) * 64); glds3(wqb + (size_t)((KS(s) * 64 + 8 * w) * 3072), v_ + g * 128, v_ + gk * 128, v_ + gv_ * 128, lds0 + WR_OFF + s * WSLOT + w * 3072);
                              glds1(x + (size_t)(64 * i + 8 * w) * 1024 + KS(s) * 128, xv0 ^ ((w & 1) * 64), lds0 + X_OFF + s * XKS + (16 + 8 * w) * 128); };
    if (TCHB) touch(wqb + blockIdx.x * 6144, (w * 6 + (lane < 6 ? lane : 5)) * 128, lds0 + GATE_OFF + 512);
    if (TCHB > 1) touch(x + blockIdx.x * 8192, (w * 8 + (lane & 7)) * 128, lds0 + GATE_OFF + 512);
    first(0);
    CODE_PREFETCH();
    first(1);
    if (TCH) touch(wqa + blockIdx.x * 6144, (w * 6 + (lane < 6 ? lane : 5)) * 128, lds0 + GATE_OFF + 512);
#define SETW(n, v) do { if (lane == 0) __hip_atomic_store((LAS int*)(lds + PROG_OFF) + (n), (v), __ATOMIC_RELAXED, __HIP_MEMORY_SCOPE_WORKGROUP); } while (0)
#define WAITW(n, v) do { while (!__all(__hip_atomic_load((LAS int*)(lds + PROG_OFF) + (n) + (lane & 3), __ATOMIC_RELAXED, __HIP_MEMORY_SCOPE_WORKGROUP) >= (v))) __builtin_amdgcn_s_sleep(1); asm volatile("" ::: "memory"); } while (0)
    const char* kvsrc = ws + WS_KV + h * 131072 + wq * 8192;
#define KVLD(b) do { glds4c(kvsrc + (b) * 32768, lane16, lds0 + (b) * 32768 + wq * 8192); glds4c(kvsrc + (b) * 32768 + 4096, lane16, lds0 + (b) * 32768 + wq * 8192 + 4096); } while (0)
    if (w >= 4) {
        __builtin_amdgcn_s_setprio(LPRIO);
        const unsigned wv0 = wvl + 16 * wq * 3072, wv1 = (wv0 ^ 64) + 8 * 3072;
        const unsigned wdst = lds0 + WR_OFF + wq * 6144;
        auto issue = [&](auto S) { constexpr int s = S.v;
            if constexpr (s == 8 || s == 9) {
                const int st = 4 * (s - 8) + wq; const char* hs = x + (size_t)(i ? 64 * i - 16 : 0) * 1024 + KS(st) * 128;
                glds1(hs, xv0, lds0 + X_OFF + st * XKS); glds1(hs, xv1, lds0 + X_OFF + st * XKS + 1024); }
            const char* wsrc = (s < 8 ? wqb : wqa) + (size_t)(KS(s) * 64 * 3072);
            const int c1 = s < 8 ? gk : g, c2 = s < 8 ? gv_ : g;
            glds3(wsrc, wv0 + g * 128, wv0 + c1 * 128, wv0 + c2 * 128, wdst + (s % 3) * WSLOT); glds3(wsrc, wv1 + g * 128, wv1 + c1 * 128, wv1 + c2 * 128, wdst + (s % 3) * WSLOT + 3072); };
        wait_bar<5 + TCH>();
        if (w == 4) glds1(gw, lane16, lds0 + GW_OFF);
        if (w == 5) { const void* sbase = ws + WS_EPOCH; const unsigned voff = (lane & 1) * 4, ldsb = lds0 + EP_OFF; unsigned sv; asm volatile(GLDS_OPEN "global_load_lds_dword %1, %2 sc1\n\ts_mov_b32 m0, %0" GLDS_ARGS); }
        issue(IC<2>{});
        f32x4 ga = {};
        unsigned sink;
        const lds_cptr gwrd = L + GW_OFF + lg * 16;
        static_for<0, 14>([&](auto S) { constexpr int s = S.v;
            constexpr int cnt[14] = {6, 6, 6, 6, 6, 6, 8, 63, 6, 6, 6, 6, 6, 6};
            TMS(s, cnt[s]);
            if constexpr (SPF >= 0 && s > SPF && s < 4) asm volatile("s_waitcnt vmcnt(%0)\n\ts_barrier" :: "i"(cnt[s]) : "memory"); else wait_bar<cnt[s]>();
            if constexpr (SPF >= 0 && s == 4) asm volatile("" :: "s"(sink));
            u32x4 g0, g1, g2, g3;
            if constexpr (s >= 4 && s < 12) if (g == (i & 7)) { constexpr int c = s - 4;
                g0 = lds128(gwrd + KS(c) * 128); g1 = lds128(xrd0 + c * XKS + (1 + wq) * 2048); g2 = lds128(gwrd + KS(c) * 128 + 64); g3 = lds128(xrd1 + c * XKS + (1 + wq) * 2048); }
            if constexpr (s + 3 < 16) issue(IC<s + 3>{});
            if constexpr (s == SPF) {
                asm volatile(".set pfo, 0\n\t.rept 24\n\ts_load_dword %0, %1, pfo\n\t.set pfo, pfo + 1024\n\t.endr" : "=&s"(sink) : "s"(wqa + g * 128 + ((i & 15) * 4 + wq) * 24576) : "memory"); }
            if constexpr (s == 7) tag = mktag(*(LAS const unsigned long long*)(lds + EP_OFF));
            if constexpr (s >= 4 && s < 12) if (g == (i & 7)) { ga = mfma16(g0, g1, ga); ga = mfma16(g2, g3, ga); }
            if constexpr (s == 7) wait_bar<14>();
                    });
        VM0(); SETW(4 + wq, 1);
        { gu64* f0 = (gu64*)(ws + WS_KVFLAG) + (((h - 4 * (lane < 32 ? GKS : GVS)) & 31) * 32 + (lane & 31)) * FS; gu64* f1 = (gu64*)(ws + WS_KVFLAG) + (h * 32 + 4 * j + wq) * FS;
#pragma unroll 1
          for (unsigned spins = 0; spins < kSpinCap; ++spins) {
              const unsigned long long a = __hip_atomic_load(f0, RLX_AGENT), b = __hip_atomic_load(f1, RLX_AGENT);
              if (__all(a == tag && b == tag)) break;
              __builtin_amdgcn_s_sleep(PSL); }
          __builtin_amdgcn_fence(__ATOMIC_ACQUIRE, "wavefront"); }
        STAMP(4);
        KVLD(0);
        WAITW(0, 1);
        glds2c(ws + WS_Q + (h * 64 + 8 * j + 2 * wq) * 1024, lane16, lds0 + SPARE_OFF + wq * 2048); KVLD(1);
        STAMP(2);
        wait_bar<0>();
        if (!KVOLD) { KVLD(2); KVLD(3); }
        if (g == (i & 7) && lane < 16) __hip_atomic_store((gu64*)(ws + WS_GATE) + 64 * i + 16 * wq + lane, (tag & 0xffffffff00000000ull) | __float_as_uint(1.f / (1.f + __expf(-ga[0]))), RLX_AGENT);
        STAMP(3);
    } else {
        const char* xsrc = x + (size_t)(64 * i + 16 * w) * 1024; const unsigned xdst = lds0 + X_OFF + (16 + 16 * w) * 128;
#define XLD(s) do { glds1(xsrc + KS(s) * 128, xv0, xdst + (s) * XKS); glds1(xsrc + KS(s) * 128, xv1, xdst + (s) * XKS + 1024); } while (0)
        { const unsigned ov = (l15 == 0 || l15 == 4) ? 0x3F803F80u : 0u; *(LAS u32x4*)(lds + ONES_OFF + tid * 16) = (u32x4){ov, ov, ov, ov}; }
        if (tid < 24) *(LAS int*)(lds + PROG_OFF + tid * 4) = 0;
        const lds_cptr wrd = L + WR_OFF + lg * 3072 + ((lane >> 2) & 3) * 128 + ((w ^ (((lane >> 3) & 1) | ((lg & 1) << 1))) * 32) + (lane & 3) * 8;
        auto readf = [&](auto S, Frags& f) { constexpr int s = S.v;
#pragma unroll
            for (int cc = 0; cc < 2; ++cc) {
#pragma unroll
                for (int sec = 0; sec < 3; ++sec) f.w[cc][sec] = tr128(wrd + (s % 3) * WSLOT + cc * 12288 + sec * 1024);
#pragma unroll
                for (int t = (s < 8 ? 1 : 0); t < 5; ++t) f.x[cc][t] = lds128((cc ? xrd1 : xrd0) + (s & 7) * XKS + t * 2048);
            } };
        f32x4 q[5] = {}, k[5] = {}, v[5] = {};
        Frags fr[2];
        wait_bar<5 + TCH>();
        XLD(2); XLD(3);
        readf(IC<0>{}, fr[0]);
        auto mm = [&](auto S, const Frags& f) { constexpr int s = S.v;
#pragma unroll
            for (int cc = 0; cc < 2; ++cc)
#pragma unroll
                for (int t = (s < 8 ? 1 : 0); t < 5; ++t) { k[t] = mfma16(f.w[cc][1], f.x[cc][t], k[t]); v[t] = mfma16(f.x[cc][t], f.w[cc][2], v[t]); if (t) q[t] = mfma16(f.w[cc][0], f.x[cc][t], q[t]); } };
        static_for<0, 16>([&](auto S) { constexpr int s = S.v;
            constexpr int cnt[14] = {4 + TCH, 4, 4, 4, 4, 2, 0, 63, 63, 63, 63, 63, 63, 63};
            if constexpr (s == 7) {
                BAR(); SBAR();
                mm(S, fr[1]);
                const unsigned dst = WS_KV + w * 131072 + (i >> 1) * 8192 + (i & 1) * 2048;
#pragma unroll
                for (int t = 0; t < 2; ++t) {
                    STC(join16(pack4(k[1 + 2 * t]), pack4(k[2 + 2 * t])), dst + gk * 524288 + t * 1024);
                    STC(join16(pack4(v[1 + 2 * t]), pack4(v[2 + 2 * t])), dst + gv_ * 524288 + 4096 + t * 1024);
                    STC(join16(pack4(q[1 + 2 * t] * QSCALE), pack4(q[2 + 2 * t] * QSCALE)), WS_Q + ((4 * g + w) * 64 + 2 * i + t) * 1024); }
#pragma unroll
                for (int t = 0; t < 5; ++t) q[t] = k[t] = v[t] = (f32x4){};
                STAMP(1); SBAR();
            }
            if constexpr (s < 14) { TMS(s, cnt[s]); wait_bar<cnt[s]>(); }
            else if constexpr (s == 14) { asm volatile("s_waitcnt lgkmcnt(0)" ::: "memory"); TMS(s, 63); SETW(w, 1); WAITW(4, 1); }
            else { TMS(s, 63); }
            SBAR();
            if constexpr (s < 15) readf(IC<s + 1>{}, fr[(s + 1) & 1]);
            if constexpr (s != 7) mm(S, fr[s & 1]);
            if constexpr (s < 15 && s != 7) {
#pragma unroll
                for (int u = 0; u < 22; ++u) { __builtin_amdgcn_sched_group_barrier(0x8, 1, 0); __builtin_amdgcn_sched_group_barrier(0x100, 1, 0); }
                __builtin_amdgcn_sched_group_barrier(0x8, 8, 0); }
            SBAR();
            if constexpr (s < 4) XLD(s + 4);
            if constexpr (s == 4) tag = mktag(*(LAS const unsigned long long*)(lds + EP_OFF));
            if constexpr (s == PUBS) { VM0(); if (lane == 0) __hip_atomic_store((gu64*)(ws + WS_KVFLAG) + ((4 * g + w) * 32 + i) * FS, tag, RLX_AGENT); }
        });
        if (!LAB) { BAR(); __builtin_amdgcn_s_setprio(3); }
        STAMP(2);
        u32x2 kf[5], vf[5], of[4];
        f32x4 mc, mp, mp0;
#pragma unroll
        for (int r = 0; r < 4; ++r) { const int dc = l15 - (4 * lg + r); mc[r] = (dc >= 0 && !(dc & 1)) ? 0.f : -3e38f; mp[r] = (dc <= -2 && !(dc & 1)) ? 0.f : -3e38f; mp0[r] = i ? mp[r] : -3e38f; }
#pragma unroll
        for (int t = 0; t < 5; ++t) { kf[t] = pack4(k[t]); vf[t] = pack4(v[t]); }
        const u32x2 one2 = {0x3F803F80u, 0x3F803F80u};
#pragma unroll
        for (int sb = 0; sb < 4; ++sb) {
            const f32x4 z = {}; const u32x2 qs = pack4(q[sb + 1] * QSCALE);
            f32x4 sp = mfma16k16(kf[sb], qs, sb ? mp : mp0), sc = mfma16k16(kf[sb + 1], qs, mc);
            float m = mx(mx(mx(mx(sc[0], sc[1]), sc[2]), mx(mx(sc[3], sp[0]), sp[1])), mx(sp[2], sp[3]));
            m = xmax32(xmax16(m));
#pragma unroll
            for (int r = 0; r < 4; ++r) { sc[r] = __builtin_amdgcn_exp2f(sc[r] - m); sp[r] = __builtin_amdgcn_exp2f(sp[r] - m); }
            const u32x2 pp = pack4(sp), pc = pack4(sc);
            f32x4 l = mfma16k16(one2, pp, z); l = mfma16k16(one2, pc, l);
            f32x4 o = mfma16k16(vf[sb], pp, z); o = mfma16k16(vf[sb + 1], pc, o);
            o *= __builtin_amdgcn_rcpf(l[0]);
            of[sb] = pack4(o);
        }
        STC(join16(of[0], of[1]), WS_OA + (2 * i * 32 + 4 * g + w) * 1024);
        STC(join16(of[2], of[3]), WS_OA + ((2 * i + 1) * 32 + 4 * g + w) * 1024);
        STAMP(17);
        if (LAB) { BAR(); __builtin_amdgcn_s_setprio(3); }
        if (KVOLD) { KVLD(2); KVLD(3); }
        STAMP(3);
    }
    STAMP(5);
    const u32x4 qa = lds128(L + SPARE_OFF + wq * 2048 + lane16), qb = lds128(L + SPARE_OFF + wq * 2048 + 1024 + lane16);
    const int tt = w & 1, kq = w >> 1, kh = w >> 2;
    const unsigned osrc = ((2 * i3 + tt) * 32 + 8 * kq) * 1024;
    const unsigned ovoff = (lane >> 3) * 1024 + g3 * 128 + (((lane & 7) ^ (((lane >> 4) & 1) << 2)) * 16);
    u32x4 of[8];
    unsigned long long gg, fb;
    gu64* obf = (gu64*)(ws + WS_OBFLAG) + ((2 * i3 + tt) * 32 + 8 * kq) * FS;
#define POLLB() asm volatile("s_nop 4\n\tglobal_load_dwordx2 %0, %1, %2 sc1" : "=v"(fb) : "v"((lane & 7) * 8 * FS), "s"(obf) : "memory")
    float gv;
    const lds_cptr wo0 = L + kq * 16384 + (8 * (lane >> 5) + ((lane >> 2) & 3)) * 128 + (((lane >> 3) & 1) * 64) + (lg & 1) * 32 + (lane & 3) * 8;
    const lds_cptr wo1 = L + kq * 16384 + (8 * (lane >> 5) + ((lane >> 2) & 3)) * 128 + ((1 ^ ((lane >> 3) & 1)) * 64) + (lg & 1) * 32 + (lane & 3) * 8;
    f32x16 a0 = {}, a1 = {};
    auto proj_a = [&]() {
#pragma unroll
            for (int u = 0; u < 8; ++u) { a0 = mfma32(tr128(wo0 + u * 2048), of[u], a0); a1 = mfma32(tr128(wo1 + u * 2048), of[u], a1); }
            a0 *= gv; a1 *= gv; PIN(a0); PIN(a1); };
    const int sidx = (int)0xDDDDDDDDu;
    const bool real = (lane & 16) == 0;
    auto finish = [&](f32x16 o, const u32x4 qf, int T) {
        float chk = o[8];
#pragma unroll
        for (int r = 0; r < 8; ++r) chk = fmaxf(chk, fabsf(o[r]) * 0x1p-30f);
        if (__builtin_expect(__any(!(o[8] < 0x1p90f && chk < 0x1p90f)), 0)) {
            const f32x16 zero = {}; float m = -3e38f;
            const unsigned ksrc = WS_KV + h * 131072;
#pragma unroll 1
            for (int t = 0; t < 64; ++t) { f32x16 s = mfma32(LDC(ksrc + (t >> 2) * 8192 + (t & 3) * 1024), qf, zero);
#pragma unroll
                for (int r = 0; r < 16; ++r) m = fmaxf(m, s[r]); }
            m = xmax32(m); o = zero;
            const unsigned vl = (lane >> 5) * 512 + l15 * 16;
            const u32x4 ones = {0x3F803F80u, 0x3F803F80u, 0x3F803F80u, 0x3F803F80u};
#pragma unroll 1
            for (int t = 0; t < 64; ++t) { const unsigned ts = ksrc + (t >> 2) * 8192 + (t & 3) * 1024; f32x16 s = mfma32(LDC(ts), qf, zero);
                const u32x4 a = __builtin_amdgcn_raw_buffer_load_b128(wsr, vl, ts + 4096, 16), c = __builtin_amdgcn_raw_buffer_load_b128(wsr, vl + 256, ts + 4096, 16);
#pragma unroll
                for (int r = 0; r < 16; ++r) s[r] = __builtin_amdgcn_exp2f(s[r] - m);
                o = smf(real ? a : ones, lo8(s), o, sidx); o = smf(real ? c : ones, hi8(s), o, sidx); }
        }
        const float rl = __builtin_amdgcn_rcpf(o[8]);
        STC(join32(cvtpk(o[0] * rl, o[1] * rl), cvtpk(o[2] * rl, o[3] * rl), cvtpk(o[4] * rl, o[5] * rl), cvtpk(o[6] * rl, o[7] * rl)), WS_OB + (T * 32 + h) * 1024);
    };
    {
        unsigned kba = lds0 + kh * 8192 + lane16, vba = lds0 + (real ? kh * 8192 + 4096 : ONES_OFF) + (lane >> 5) * 512 + l15 * 16;
        const int vstep = real ? 16384 : 0;
        f32x16 sa, sb, ca, cb, oa = {}, ob = {};
        u32x4 kf = lds128(L + lane16), va = lds128((lds_cptr)(uintptr_t)vba), vc = va;
        { const f32x16 zero = {}; sa = mfma32(kf, qa, zero); sb = mfma32(kf, qb, zero); kf = lds128((lds_cptr)(uintptr_t)kba);
          float ma = sa[0], mb = sb[0];
#pragma unroll
          for (int r = 1; r < 16; ++r) { ma = mx(ma, sa[r]); mb = mx(mb, sb[r]); }
          ma = xmax32(ma); mb = xmax32(mb);
#pragma unroll
          for (int r = 0; r < 16; ++r) { ca[r] = -ma; cb[r] = -mb; sb[r] = 0.f; } }
        PIN(ca); PIN(cb);
        sa = mfma32(kf, qa, ca);
        auto run = [&](int n) {
            asm volatile("s_waitcnt lgkmcnt(0)\n\ts_nop 11\n"
            "1:\n\t"
            "v_exp_f32 v32, v32\n\tv_exp_f32 v33, v33\n\tv_smfmac_f32_32x32x32_bf16 %[ob], %[vc], v[56:63], %[ix]\n\tds_read_b128 %[vc], %[vb] offset:256\n\tv_exp_f32 v34, v34\n\tv_exp_f32 v35, v35\n\tv_exp_f32 v36, v36\n\tv_exp_f32 v37, v37\n\tv_mfma_f32_32x32x16_bf16 v[48:63], %[kf], %[qb], %[cb]\n\tds_read_b128 %[kf], %[kb] offset:1024\n\tv_exp_f32 v38, v38\n\tv_exp_f32 v39, v39\n\tv_exp_f32 v40, v40\n\tv_exp_f32 v41, v41\n\ts_waitcnt lgkmcnt(2)\n\tv_smfmac_f32_32x32x32_bf16 %[oa], %[va], v[32:39], %[ix]\n\tv_exp_f32 v42, v42\n\tv_exp_f32 v43, v43\n\tv_exp_f32 v44, v44\n\tv_exp_f32 v45, v45\n\tv_exp_f32 v46, v46\n\tv_exp_f32 v47, v47\n\t"
            "v_exp_f32 v48, v48\n\tv_exp_f32 v49, v49\n\ts_waitcnt lgkmcnt(0)\n\tv_smfmac_f32_32x32x32_bf16 %[oa], %[vc], v[40:47], %[ix]\n\tv_exp_f32 v50, v50\n\tv_exp_f32 v51, v51\n\tv_exp_f32 v52, v52\n\tv_exp_f32 v53, v53\n\tv_mfma_f32_32x32x16_bf16 v[32:47], %[kf], %[qa], %[ca]\n\tv_exp_f32 v54, v54\n\tv_exp_f32 v55, v55\n\tv_exp_f32 v56, v56\n\tv_exp_f32 v57, v57\n\tv_smfmac_f32_32x32x32_bf16 %[ob], %[va], v[48:55], %[ix]\n\tds_read_b128 %[va], %[vb] offset:1024\n\tv_exp_f32 v58, v58\n\tv_exp_f32 v59, v59\n\tv_exp_f32 v60, v60\n\tv_exp_f32 v61, v61\n\tv_exp_f32 v62, v62\n\tv_exp_f32 v63, v63\n\t"
            "v_exp_f32 v32, v32\n\tv_exp_f32 v33, v33\n\tv_smfmac_f32_32x32x32_bf16 %[ob], %[vc], v[56:63], %[ix]\n\tds_read_b128 %[vc], %[vb] offset:1280\n\tv_exp_f32 v34, v34\n\tv_exp_f32 v35, v35\n\tv_exp_f32 v36, v36\n\tv_exp_f32 v37, v37\n\tv_mfma_f32_32x32x16_bf16 v[48:63], %[kf], %[qb], %[cb]\n\tds_read_b128 %[kf], %[kb] offset:2048\n\tv_exp_f32 v38, v38\n\tv_exp_f32 v39, v39\n\tv_exp_f32 v40, v40\n\tv_exp_f32 v41, v41\n\ts_waitcnt lgkmcnt(2)\n\tv_smfmac_f32_32x32x32_bf16 %[oa], %[va], v[32:39], %[ix]\n\tv_exp_f32 v42, v42\n\tv_exp_f32 v43, v43\n\tv_exp_f32 v44, v44\n\tv_exp_f32 v45, v45\n\tv_exp_f32 v46, v46\n\tv_exp_f32 v47, v47\n\t"
            "v_exp_f32 v48, v48\n\tv_exp_f32 v49, v49\n\ts_waitcnt lgkmcnt(0)\n\tv_smfmac_f32_32x32x32_bf16 %[oa], %[vc], v[40:47], %[ix]\n\tv_exp_f32 v50, v50\n\tv_exp_f32 v51, v51\n\tv_exp_f32 v52, v52\n\tv_exp_f32 v53, v53\n\tv_mfma_f32_32x32x16_bf16 v[32:47], %[kf], %[qa], %[ca]\n\tv_exp_f32 v54, v54\n\tv_exp_f32 v55, v55\n\tv_exp_f32 v56, v56\n\tv_exp_f32 v57, v57\n\tv_smfmac_f32_32x32x32_bf16 %[ob], %[va], v[48:55], %[ix]\n\tds_read_b128 %[va], %[vb] offset:2048\n\tv_exp_f32 v58, v58\n\tv_exp_f32 v59, v59\n\tv_exp_f32 v60, v60\n\tv_exp_f32 v61, v61\n\tv_exp_f32 v62, v62\n\tv_exp_f32 v63, v63\n\t"
            "v_exp_f32 v32, v32\n\tv_exp_f32 v33, v33\n\tv_smfmac_f32_32x32x32_bf16 %[ob], %[vc], v[56:63], %[ix]\n\tds_read_b128 %[vc], %[vb] offset:2304\n\tv_exp_f32 v34, v34\n\tv_exp_f32 v35, v35\n\tv_exp_f32 v36, v36\n\tv_exp_f32 v37, v37\n\tv_mfma_f32_32x32x16_bf16 v[48:63], %[kf], %[qb], %[cb]\n\tds_read_b128 %[kf], %[kb] offset:3072\n\tv_exp_f32 v38, v38\n\tv_exp_f32 v39, v39\n\tv_exp_f32 v40, v40\n\tv_exp_f32 v41, v41\n\ts_waitcnt lgkmcnt(2)\n\tv_smfmac_f32_32x32x32_bf16 %[oa], %[va], v[32:39], %[ix]\n\tv_exp_f32 v42, v42\n\tv_exp_f32 v43, v43\n\tv_exp_f32 v44, v44\n\tv_exp_f32 v45, v45\n\tv_exp_f32 v46, v46\n\tv_exp_f32 v47, v47\n\t"
            "v_exp_f32 v48, v48\n\tv_exp_f32 v49, v49\n\ts_waitcnt lgkmcnt(0)\n\tv_smfmac_f32_32x32x32_bf16 %[oa], %[vc], v[40:47], %[ix]\n\tv_exp_f32 v50, v50\n\tv_exp_f32 v51, v51\n\tv_exp_f32 v52, v52\n\tv_exp_f32 v53, v53\n\tv_mfma_f32_32x32x16_bf16 v[32:47], %[kf], %[qa], %[ca]\n\tv_exp_f32 v54, v54\n\tv_exp_f32 v55, v55\n\tv_exp_f32 v56, v56\n\tv_exp_f32 v57, v57\n\tv_smfmac_f32_32x32x32_bf16 %[ob], %[va], v[48:55], %[ix]\n\tds_read_b128 %[va], %[vb] offset:3072\n\tv_exp_f32 v58, v58\n\tv_exp_f32 v59, v59\n\tv_exp_f32 v60, v60\n\tv_exp_f32 v61, v61\n\tv_exp_f32 v62, v62\n\tv_exp_f32 v63, v63\n\t"
            "v_exp_f32 v32, v32\n\tv_exp_f32 v33, v33\n\tv_smfmac_f32_32x32x32_bf16 %[ob], %[vc], v[56:63], %[ix]\n\tds_read_b128 %[vc], %[vb] offset:3328\n\tv_exp_f32 v34, v34\n\tv_exp_f32 v35, v35\n\tv_exp_f32 v36, v36\n\tv_exp_f32 v37, v37\n\tv_mfma_f32_32x32x16_bf16 v[48:63], %[kf], %[qb], %[cb]\n\tds_read_b128 %[kf], %[kb] offset:16384\n\tv_exp_f32 v38, v38\n\tv_exp_f32 v39, v39\n\tv_exp_f32 v40, v40\n\tv_exp_f32 v41, v41\n\ts_waitcnt lgkmcnt(2)\n\tv_smfmac_f32_32x32x32_bf16 %[oa], %[va], v[32:39], %[ix]\n\tv_exp_f32 v42, v42\n\tv_exp_f32 v43, v43\n\tv_exp_f32 v44, v44\n\tv_exp_f32 v45, v45\n\tv_exp_f32 v46, v46\n\tv_exp_f32 v47, v47\n\t"
            "v_exp_f32 v48, v48\n\tv_exp_f32 v49, v49\n\ts_waitcnt lgkmcnt(0)\n\tv_smfmac_f32_32x32x32_bf16 %[oa], %[vc], v[40:47], %[ix]\n\tv_exp_f32 v50, v50\n\tv_exp_f32 v51, v51\n\tv_exp_f32 v52, v52\n\tv_exp_f32 v53, v53\n\tv_mfma_f32_32x32x16_bf16 v[32:47], %[kf], %[qa], %[ca]\n\tv_exp_f32 v54, v54\n\tv_exp_f32 v55, v55\n\tv_exp_f32 v56, v56\n\tv_exp_f32 v57, v57\n\tv_add_u32 %[vb], %[vb], %[vs]\n\tv_smfmac_f32_32x32x32_bf16 %[ob], %[va], v[48:55], %[ix]\n\tds_read_b128 %[va], %[vb] offset:0\n\tv_add_u32 %[kb], 0x4000, %[kb]\n\ts_sub_u32 %[n], %[n], 1\n\tv_exp_f32 v58, v58\n\tv_exp_f32 v59, v59\n\tv_exp_f32 v60, v60\n\tv_exp_f32 v61, v61\n\tv_exp_f32 v62, v62\n\tv_exp_f32 v63, v63\n\t"
            "s_cmp_lg_u32 %[n], 0\n\ts_cbranch_scc1 1b\n\ts_waitcnt lgkmcnt(0)\n\ts_nop 7"
            : "+{v[32:47]}"(sa), "+{v[48:63]}"(sb), [oa] "+v"(oa), [ob] "+v"(ob), [kf] "+v"(kf), [va] "+v"(va), [vc] "+v"(vc), [kb] "+v"(kba), [vb] "+v"(vba), [n] "+s"(n)
            : [qa] "v"(qa), [qb] "v"(qb), [ca] "v"(ca), [cb] "v"(cb), [vs] "v"(vstep), [ix] "v"(sidx) : "scc", "memory");
        };
        STAMP(12);
        run(2);
        __builtin_amdgcn_s_setprio(2);
        run(2);
        STAMP(13);
        if (w < 4) { VM0(); if (lane == 0) __hip_atomic_store((gu64*)(ws + WS_OAFLAG) + (i * 32 + 4 * g + w) * FS, tag, RLX_AGENT); }
        SETW(8 + w, 1);
        WAITW(8, 1);
        kf = lds128((lds_cptr)(uintptr_t)kba); va = lds128((lds_cptr)(uintptr_t)vba); sa = mfma32(kf, qa, ca);
        if (w >= 4) WAITW(12, 1);
#pragma unroll
        for (int u = 0; u < 8; ++u) glds1(woa + (size_t)(64 * (2 * wq + kh) + 8 * u) * 1024, ovoff, lds0 + (2 * wq + kh) * 8192 + u * 1024);
        if (w < 2) touch(wob + g3 * 128, (128 * (idx & 3) + 64 * w + lane) * 1024, lds0 + GATE_OFF + 512);
        __builtin_amdgcn_s_setprio(1);
        STAMP(14);
        run(SEG3);
        STAMP(15);
#pragma unroll 1
        for (unsigned spins = 0; spins < kSpinCap; ++spins) {
            const unsigned long long f = __hip_atomic_load((gu64*)(ws + WS_OAFLAG) + (i3 * 32 + 8 * kq + (lane & 7)) * FS, RLX_AGENT);
            gg = __hip_atomic_load((gu64*)(ws + WS_GATE) + 64 * i3 + 32 * tt + (lane & 31), RLX_AGENT);
            if (__all(f == tag && (gg >> 32) == (tag >> 32))) break;
            __builtin_amdgcn_s_sleep(1); }
        __builtin_amdgcn_fence(__ATOMIC_ACQUIRE, "wavefront");
#pragma unroll
        for (int u = 0; u < 8; ++u) of[u] = LDC(WS_OA + osrc + u * 1024);
        SETW(8 + w, 2);
        __builtin_amdgcn_s_setprio(0);
        STAMP(8);
        run(4 - SEG3);
#pragma unroll
        for (int u = 0; u < 8; ++u) PIN(of[u]);
        if (w >= 4) POLLB();
        ob = smf(vc, hi8(sb), ob, sidx);
        STAMP(6);
        f32x16 give, o;
#pragma unroll
        for (int r = 0; r < 16; ++r) { give[r] = r < 9 ? (kh ? oa[r] : ob[r]) : 0.f; o[r] = r < 9 ? (kh ? ob[r] : oa[r]) : 0.f; }
        LAS char* xw = (LAS char*)lds + SPARE_OFF + w * 3072 + lane16;
        *(LAS f32x4*)xw = __builtin_shufflevector(give, give, 0, 1, 2, 3); *(LAS f32x4*)(xw + 1024) = __builtin_shufflevector(give, give, 4, 5, 6, 7); *(LAS float*)(xw + 2048) = give[8];
        gv = __uint_as_float((unsigned)gg);
        if (w < 4) {
            WAITW(8, 2); WAITW(12, 2);
            proj_a();
            SETW(8 + w, 3); WAITW(8, 3);
#pragma unroll
            for (int u = 0; u < 16; ++u) glds1(wob + (size_t)(128 * w + 8 * u) * 1024, ovoff, lds0 + (w < 2 ? w * 16384 + u * 1024 : w * 32768 + (u >> 3) * 16384 + (u & 7) * 1024));
            VM0(); POLLB(); }
        STAMP(9);
        BAR();
        STAMP(23);
        LAS const char* xr = L + SPARE_OFF + (w ^ 4) * 3072 + lane16;
        const f32x4 r0 = *(LAS const f32x4*)xr, r1 = *(LAS const f32x4*)(xr + 1024);
#pragma unroll
        for (int r = 0; r < 4; ++r) { o[r] += r0[r]; o[4 + r] += r1[r]; }
        o[8] += *(LAS const float*)(xr + 2048);
        finish(o, kh ? qb : qa, 8 * j + 2 * wq + kh);
        STAMP(7);
    }
    {
        f32x16 b0 = {}, b1 = {};
        u32x4 wf[16], ob8[8];
        if (TORD) { asm volatile("s_waitcnt vmcnt(0)" : "+v"(fb) :: "memory");
            if (lane == 0) __hip_atomic_store((gu64*)(ws + WS_OBFLAG) + ((8 * j + 2 * wq + kh) * 32 + h) * FS, tag, RLX_AGENT); }
        else asm volatile("s_waitcnt vmcnt(1)" : "+v"(fb) :: "memory");
        unsigned got = (unsigned)__builtin_amdgcn_ballot_w64(fb == tag) & 0xffu;
        const char* pb = ws + WS_OB + osrc + 4096; unsigned long long st;
        asm volatile("s_nop 4\n\t"
            "s_bitcmp1_b32 %[got], 0\n\ts_cselect_b64 %[t], %[pb], %[pa]\n\tglobal_load_dwordx4 %0, %[vo], %[t] offset:-4096 sc1\n\t"
            "s_bitcmp1_b32 %[got], 1\n\ts_cselect_b64 %[t], %[pb], %[pa]\n\tglobal_load_dwordx4 %1, %[vo], %[t] offset:-3072 sc1\n\t"
            "s_bitcmp1_b32 %[got], 2\n\ts_cselect_b64 %[t], %[pb], %[pa]\n\tglobal_load_dwordx4 %2, %[vo], %[t] offset:-2048 sc1\n\t"
            "s_bitcmp1_b32 %[got], 3\n\ts_cselect_b64 %[t], %[pb], %[pa]\n\tglobal_load_dwordx4 %3, %[vo], %[t] offset:-1024 sc1\n\t"
            "s_bitcmp1_b32 %[got], 4\n\ts_cselect_b64 %[t], %[pb], %[pa]\n\tglobal_load_dwordx4 %4, %[vo], %[t] offset:0 sc1\n\t"
            "s_bitcmp1_b32 %[got], 5\n\ts_cselect_b64 %[t], %[pb], %[pa]\n\tglobal_load_dwordx4 %5, %[vo], %[t] offset:1024 sc1\n\t"
            "s_bitcmp1_b32 %[got], 6\n\ts_cselect_b64 %[t], %[pb], %[pa]\n\tglobal_load_dwordx4 %6, %[vo], %[t] offset:2048 sc1\n\t"
            "s_bitcmp1_b32 %[got], 7\n\ts_cselect_b64 %[t], %[pb], %[pa]\n\tglobal_load_dwordx4 %7, %[vo], %[t] offset:3072 sc1\n\t"
            : "=&v"(ob8[0]), "=&v"(ob8[1]), "=&v"(ob8[2]), "=&v"(ob8[3]), "=&v"(ob8[4]), "=&v"(ob8[5]), "=&v"(ob8[6]), "=&v"(ob8[7]), [t] "=&s"(st)
            : [got] "s"(got), [pb] "s"(pb), [pa] "s"(ws + WS_OA + osrc + 4096), [vo] "v"(lane16) : "scc", "memory");
        if (!TORD) { asm volatile("s_waitcnt vmcnt(8)" ::: "memory");
            if (lane == 0) __hip_atomic_store((gu64*)(ws + WS_OBFLAG) + ((8 * j + 2 * wq + kh) * 32 + h) * FS, tag, RLX_AGENT); }
        STAMP(22);
        if (w >= 4) { proj_a(); SETW(8 + w, 3); }
        const int wb0 = kq < 2 ? 0 : kq * 16384, wb1 = kq < 2 ? 0 : kq * 16384 + 8192;
#pragma unroll
        for (int u = 0; u < 8; ++u) { wf[2 * u] = tr128(wo0 + (u < 4 ? wb0 : wb1) + u * 2048); wf[2 * u + 1] = tr128(wo1 + (u < 4 ? wb0 : wb1) + u * 2048); }
        STAMP(21);
        { unsigned fresh, spins = kSpinCap;
          asm volatile("s_nop 4\n"
            "2:\n\ts_cmp_eq_u32 %[got], 0xff\n\ts_cbranch_scc1 3f\n\t"
            "global_load_dwordx2 %[fb], %[fo], %[fp] sc1\n\ts_waitcnt vmcnt(0)\n\t"
            "v_cmp_eq_u64 vcc, %[tg], %[fb]\n\ts_nop 1\n\ts_and_b32 %[fr], vcc_lo, 0xff\n\ts_andn2_b32 %[fr], %[fr], %[got]\n\t"
            "s_bitcmp1_b32 %[fr], 0\n\ts_cbranch_scc0 10f\n\tglobal_load_dwordx4 %0, %[vo], %[pb] offset:-4096 sc1\n10:\n\t"
            "s_bitcmp1_b32 %[fr], 1\n\ts_cbranch_scc0 11f\n\tglobal_load_dwordx4 %1, %[vo], %[pb] offset:-3072 sc1\n11:\n\t"
            "s_bitcmp1_b32 %[fr], 2\n\ts_cbranch_scc0 12f\n\tglobal_load_dwordx4 %2, %[vo], %[pb] offset:-2048 sc1\n12:\n\t"
            "s_bitcmp1_b32 %[fr], 3\n\ts_cbranch_scc0 13f\n\tglobal_load_dwordx4 %3, %[vo], %[pb] offset:-1024 sc1\n13:\n\t"
            "s_bitcmp1_b32 %[fr], 4\n\ts_cbranch_scc0 14f\n\tglobal_load_dwordx4 %4, %[vo], %[pb] offset:0 sc1\n14:\n\t"
            "s_bitcmp1_b32 %[fr], 5\n\ts_cbranch_scc0 15f\n\tglobal_load_dwordx4 %5, %[vo], %[pb] offset:1024 sc1\n15:\n\t"
            "s_bitcmp1_b32 %[fr], 6\n\ts_cbranch_scc0 16f\n\tglobal_load_dwordx4 %6, %[vo], %[pb] offset:2048 sc1\n16:\n\t"
            "s_bitcmp1_b32 %[fr], 7\n\ts_cbranch_scc0 17f\n\tglobal_load_dwordx4 %7, %[vo], %[pb] offset:3072 sc1\n17:\n\t"
            "s_or_b32 %[got], %[got], %[fr]\n\ts_sleep 1\n\ts_sub_u32 %[sp], %[sp], 1\n\ts_cbranch_scc0 2b\n"
            "3:\n\ts_waitcnt vmcnt(0)"
            : "+v"(ob8[0]), "+v"(ob8[1]), "+v"(ob8[2]), "+v"(ob8[3]), "+v"(ob8[4]), "+v"(ob8[5]), "+v"(ob8[6]), "+v"(ob8[7]), [got] "+s"(got), [fr] "=&s"(fresh), [sp] "+s"(spins), [fb] "=&v"(fb)
            : [pb] "s"(pb), [vo] "v"(lane16), [fo] "v"((lane & 7) * 8 * FS), [fp] "s"(obf), [tg] "s"(tag) : "scc", "vcc", "memory"); }
        STAMP(16);
        STAMP(10);
#pragma unroll
        for (int u = 0; u < 8; ++u) { b0 = mfma32(wf[2 * u], ob8[u], b0); b1 = mfma32(wf[2 * u + 1], ob8[u], b1); }
        a0 += b0 * (1.f - gv); a1 += b1 * (1.f - gv);
        STAMP(18);
        WAITW(12, 3);
        const int cs = tt ? 1024 : 512;
        LAS f32x4* rw = (LAS f32x4*)(lds + (tt ? 73728 : 32768) + kq * 2048 + lane16);
        if (kq != 0) { rw[0] = __builtin_shufflevector(a0, a0, 0, 1, 2, 3); rw[64] = __builtin_shufflevector(a0, a0, 4, 5, 6, 7); }
        if (kq != 1) { rw[cs] = __builtin_shufflevector(a0, a0, 8, 9, 10, 11); rw[cs + 64] = __builtin_shufflevector(a0, a0, 12, 13, 14, 15); }
        if (kq != 2) { rw[2 * cs] = __builtin_shufflevector(a1, a1, 0, 1, 2, 3); rw[2 * cs + 64] = __builtin_shufflevector(a1, a1, 4, 5, 6, 7); }
        if (kq != 3) { rw[3 * cs] = __builtin_shufflevector(a1, a1, 8, 9, 10, 11); rw[3 * cs + 64] = __builtin_shufflevector(a1, a1, 12, 13, 14, 15); }
        const f32x16 am = kq < 2 ? a0 : a1; const f32x8 own = (kq & 1) ? hi8(am) : lo8(am);
        STAMP(19);
        BAR();
        STAMP(20);
        if (blockIdx.x == 0 && tid == 0) __hip_atomic_store((gu64*)(ws + WS_EPOCH), tag, RLX_AGENT);
        LAS const f32x4* rr = (LAS const f32x4*)(lds + (tt ? 73728 : 32768) + kq * cs * 16 + lane16);
        const int s1 = ((kq + 1) & 3) * 128, s2 = ((kq + 2) & 3) * 128, s3 = ((kq + 3) & 3) * 128;
        const f32x4 r0 = (__builtin_shufflevector(own, own, 0, 1, 2, 3) + rr[s1]) + (rr[s2] + rr[s3]), r1 = (__builtin_shufflevector(own, own, 4, 5, 6, 7) + rr[s1 + 64]) + (rr[s2 + 64] + rr[s3 + 64]);
        *(u32x4*)(out + (size_t)(64 * i3 + 32 * tt + (lane & 31)) * 1024 + g3 * 128 + kq * 32 + (lane >> 5) * 16) =
            join32(cvtpk(r0[0], r0[1]), cvtpk(r0[2], r0[3]), cvtpk(r1[0], r1[1]), cvtpk(r1[2], r1[3]));
    }
    STAMP(11);
    STAMP_DUMP();
}
}

extern "C" void kernel_launch(void* const* d_in, const int* in_sizes, int n_in, void* d_out, int out_size, void* d_ws, size_t ws_size, hipStream_t stream) {
    fused_kernel<<<256, 512, 0, stream>>>((const char*)d_in[0], (const char*)d_in[1], (const char*)d_in[2], (const char*)d_in[4], (const char*)d_in[5],
                                          (const char*)d_in[6], (char*)d_out, (char*)d_ws);
}
```

```cpp
#include <hip/hip_runtime.h>
#include <hip/hip_bf16.h>
#include <stdint.h>

#define GAS __attribute__((address_space(1)))
#define LAS __attribute__((address_space(3)))
#define RLX_AGENT __ATOMIC_RELAXED, __HIP_MEMORY_SCOPE_AGENT
typedef unsigned u32x2 __attribute__((ext_vector_type(2)));
typedef unsigned u32x4 __attribute__((ext_vector_type(4)));
typedef float f32x2 __attribute__((ext_vector_type(2)));
typedef float f32x4 __attribute__((ext_vector_type(4)));
typedef float f32x8 __attribute__((ext_vector_type(8)));
typedef float f32x16 __attribute__((ext_vector_type(16)));
typedef __bf16 bf16x2 __attribute__((ext_vector_type(2)));
typedef __bf16 bf16x8 __attribute__((ext_vector_type(8)));
typedef __bf16 bf16x16 __attribute__((ext_vector_type(16)));
typedef short s16x4 __attribute__((ext_vector_type(4)));
typedef GAS unsigned long long gu64;
typedef LAS const char* lds_cptr;

namespace {
constexpr unsigned WS_EPOCH = 0;
constexpr int FS = 8;
constexpr unsigned WS_KVFLAG = 11u << 20;
constexpr unsigned WS_OAFLAG = WS_KVFLAG + 1024 * FS * 8;
constexpr unsigned WS_OBFLAG = WS_OAFLAG + 1024 * FS * 8;
constexpr unsigned WS_GATE = 294912;
constexpr unsigned WS_Q = 1u << 20;
constexpr unsigned WS_KV = WS_Q + (2u << 20);
constexpr unsigned WS_OA = WS_KV + (4u << 20);
constexpr unsigned WS_OB = WS_OA + (2u << 20);
constexpr unsigned WS_END = WS_OB + (2u << 20);
static_assert(WS_END <= WS_KVFLAG, "ws");
constexpr int X_OFF = 0, XKS = 80 * 128, WR_OFF = 8 * XKS, WSLOT = 24576;
constexpr int GW_OFF = WR_OFF + 3 * WSLOT, EP_OFF = GW_OFF + 1024, PROG_OFF = EP_OFF + 256, DROP_OFF = EP_OFF + 512;
constexpr int ONES_OFF = 159744, LDS_BYTES = 163840;
constexpr int SPARE_OFF = 131072;
static_assert(DROP_OFF + 256 <= ONES_OFF && SPARE_OFF + 8 * 3072 <= GW_OFF, "lds");
constexpr float QSCALE = 0.25f * 1.4426950408889634f;
constexpr unsigned kSpinCap = 1u << 18;
constexpr int GKS = 2, GVS = 4;
constexpr int PFKB = 24;

#define SBAR() __builtin_amdgcn_sched_barrier(0)
#define PIN(x) asm volatile("" : "+v"(x))

__device__ __forceinline__ unsigned cvtpk(float lo, float hi) { return __builtin_bit_cast(unsigned, __builtin_convertvector((f32x2){lo, hi}, bf16x2)); }
#define GLDS_OPEN "s_mov_b32 %0, m0\n\ts_mov_b32 m0, %3\n\ts_nop 2\n\t"
#define GLDS_ARGS : "=&s"(sv) : "v"(voff), "s"(sbase), "s"(ldsb) : "memory"
__device__ __forceinline__ void glds1(const void* sbase, unsigned voff, unsigned ldsb) { unsigned sv; asm volatile(GLDS_OPEN "global_load_lds_dwordx4 %1, %2\n\ts_mov_b32 m0, %0" GLDS_ARGS); }
__device__ __forceinline__ void glds3(const void* sbase, unsigned voff, unsigned v1, unsigned v2, unsigned ldsb) { unsigned sv;
    asm volatile(GLDS_OPEN "global_load_lds_dwordx4 %1, %2\n\tglobal_load_lds_dwordx4 %4, %2 offset:1024\n\tglobal_load_lds_dwordx4 %5, %2 offset:2048\n\ts_mov_b32 m0, %0"
                 : "=&s"(sv) : "v"(voff), "s"(sbase), "s"(ldsb), "v"(v1), "v"(v2) : "memory"); }
__device__ __forceinline__ void glds4c(const void* sbase, unsigned voff, unsigned ldsb) { unsigned sv;
    asm volatile(GLDS_OPEN "global_load_lds_dwordx4 %1, %2 sc1\n\tglobal_load_lds_dwordx4 %1, %2 offset:1024 sc1\n\tglobal_load_lds_dwordx4 %1, %2 offset:2048 sc1\n\tglobal_load_lds_dwordx4 %1, %2 offset:3072 sc1\n\ts_mov_b32 m0, %0" GLDS_ARGS); }
__device__ __forceinline__ void touch(const void* sbase, unsigned voff, unsigned ldsb) { unsigned sv; asm volatile(GLDS_OPEN "global_load_lds_dword %1, %2\n\ts_mov_b32 m0, %0" GLDS_ARGS); }
__device__ __forceinline__ void glds2c(const void* sbase, unsigned voff, unsigned ldsb) { unsigned sv;
    asm volatile(GLDS_OPEN "global_load_lds_dwordx4 %1, %2 sc1\n\tglobal_load_lds_dwordx4 %1, %2 offset:1024 sc1\n\ts_mov_b32 m0, %0" GLDS_ARGS); }
__device__ __forceinline__ u32x2 tr64(lds_cptr p) { return __builtin_bit_cast(u32x2, __builtin_amdgcn_ds_read_tr16_b64_v4i16((LAS s16x4*)p)); }
__device__ __forceinline__ u32x4 tr128(lds_cptr p) { const u32x2 a = tr64(p), b = tr64(p + 512); return (u32x4){a[0], a[1], b[0], b[1]}; }
__device__ __forceinline__ u32x4 lds128(lds_cptr p) { return *(LAS const u32x4*)p; }
__device__ __forceinline__ f32x4 mfma16(u32x4 a, u32x4 b, f32x4 c) { return __builtin_amdgcn_mfma_f32_16x16x32_bf16(__builtin_bit_cast(bf16x8, a), __builtin_bit_cast(bf16x8, b), c, 0, 0, 0); }
__device__ __forceinline__ f32x4 mfma16k16(u32x2 a, u32x2 b, f32x4 c) { return __builtin_amdgcn_mfma_f32_16x16x16bf16_1k(__builtin_bit_cast(s16x4, a), __builtin_bit_cast(s16x4, b), c, 0, 0, 0); }
__device__ __forceinline__ f32x16 mfma32(u32x4 a, u32x4 b, f32x16 c) { return __builtin_amdgcn_mfma_f32_32x32x16_bf16(__builtin_bit_cast(bf16x8, a), __builtin_bit_cast(bf16x8, b), c, 0, 0, 0); }
__device__ __forceinline__ f32x16 smf(u32x4 a, f32x8 b, f32x16 c, int idx) { return __builtin_amdgcn_smfmac_f32_32x32x32_bf16(__builtin_bit_cast(bf16x8, a), __builtin_bit_cast(bf16x16, b), c, idx, 0, 0); }
__device__ __forceinline__ u32x2 pack4(f32x4 v) { return (u32x2){cvtpk(v[0], v[1]), cvtpk(v[2], v[3])}; }
__device__ __forceinline__ u32x4 join16(u32x2 x, u32x2 y) {
    auto a = __builtin_amdgcn_permlane16_swap(x[0], y[0], false, false); auto b = __builtin_amdgcn_permlane16_swap(x[1], y[1], false, false);
    return (u32x4){a[0], b[0], a[1], b[1]}; }
__device__ __forceinline__ u32x4 join32(unsigned a0, unsigned a1, unsigned b0, unsigned b1) {
    auto p0 = __builtin_amdgcn_permlane32_swap(a0, b0, false, false); auto p1 = __builtin_amdgcn_permlane32_swap(a1, b1, false, false);
    return (u32x4){p0[0], p1[0], p0[1], p1[1]}; }
__device__ __forceinline__ float mx(float a, float b) { return __builtin_elementwise_maximum(a, b); }
__device__ __forceinline__ float xmax16(float v) { auto r = __builtin_amdgcn_permlane16_swap(__float_as_uint(v), __float_as_uint(v), false, false); return mx(__uint_as_float(r[0]), __uint_as_float(r[1])); }
__device__ __forceinline__ float xmax32(float v) { auto r = __builtin_amdgcn_permlane32_swap(__float_as_uint(v), __float_as_uint(v), false, false); return mx(__uint_as_float(r[0]), __uint_as_float(r[1])); }
__device__ __forceinline__ f32x8 lo8(const f32x16& v) { return __builtin_shufflevector(v, v, 0, 1, 2, 3, 4, 5, 6, 7); }
__device__ __forceinline__ f32x8 hi8(const f32x16& v) { return __builtin_shufflevector(v, v, 8, 9, 10, 11, 12, 13, 14, 15); }

template <int N> struct IC { static constexpr int v = N; };
template <int B, int E, class F> __device__ __forceinline__ void static_for(F&& f) { if constexpr (B < E) { f(IC<B>{}); static_for<B + 1, E>(f); } }
template <int N> __device__ __forceinline__ void wait_bar() { asm volatile("s_waitcnt vmcnt(%0) lgkmcnt(0)\n\ts_barrier" :: "i"(N) : "memory"); }
struct Frags { u32x4 x[2][5], w[2][3]; };

__global__ __launch_bounds__(512, 2) void fused_kernel(const char* __restrict__ x, const char* __restrict__ wqa, const char* __restrict__ woa,
                                                       const char* __restrict__ wqb, const char* __restrict__ wob, const char* __restrict__ gw,
                                                       char* __restrict__ out, char* ws) {
    __shared__ __attribute__((aligned(256))) char lds[LDS_BYTES];
    const int tid = threadIdx.x, lane = tid & 63, w = __builtin_amdgcn_readfirstlane(tid >> 6);
    const int xcd = blockIdx.x & 7, idx = blockIdx.x >> 3;
    const int g = 2 * (xcd >> 1) + (idx & 1), i = 16 * (xcd & 1) + (idx >> 1);
    const int h = 4 * ((xcd + 4) & 7) + (idx & 3), j = idx >> 2;
    const int i3 = 4 * xcd + (idx & 3), g3 = idx >> 2;
    const unsigned lds0 = (unsigned)(uintptr_t)(LAS char*)lds;
    const lds_cptr L = (lds_cptr)lds;
    const __amdgpu_buffer_rsrc_t wsr = __builtin_amdgcn_make_buffer_rsrc(ws, 0, WS_END, 0x00020000);
    const int lane16 = lane * 16, l15 = lane & 15, lg = lane >> 4, wq = w & 3;
#define LDC(soff) __builtin_amdgcn_raw_buffer_load_b128(wsr, lane16, (soff), 16)
#define STC(v, soff) __builtin_amdgcn_raw_buffer_store_b128((v), wsr, lane16 + (soff), 0, 16)
#define BAR() asm volatile("s_waitcnt lgkmcnt(0)\n\ts_barrier" ::: "memory")
#define VM0() asm volatile("s_waitcnt vmcnt(0)" ::: "memory")
    const int rot = (i + 4 * (g & 1) + (g >> 1)) & 7;
#define KS(s) (((s) + rot) & 7)
    auto mktag = [&](unsigned long long ev) { const unsigned long long ep = ((unsigned long long)__builtin_amdgcn_readfirstlane((unsigned)(ev >> 32)) << 32) | (unsigned)__builtin_amdgcn_readfirstlane((unsigned)ev);
        unsigned long long z = (ep + 0x9E3779B97F4A7C15ull) * 0xBF58476D1CE4E5B9ull; z ^= z >> 29; return ((z ^ ep) >> 32) ? z : ~z; };
    unsigned long long tag;
    const int xsw = (l15 >> 1) & 7;
    const lds_cptr xrd0 = L + X_OFF + l15 * 128 + ((lg ^ xsw) * 16), xrd1 = L + X_OFF + l15 * 128 + (((lg + 4) ^ xsw) * 16);
    const unsigned xv0 = (lane >> 3) * 1024 + (((lane & 7) ^ lg) * 16), xv1 = (xv0 ^ 64) + 8192;
    const int gk = (g + GKS) & 7, gv_ = (g + GVS) & 7;
    const unsigned wvl = (lane >> 3) * 3072 + (((lane & 7) ^ ((lg & 1) << 1)) * 16);
    auto first = [&](int s) { const unsigned v_ = wvl ^ ((w & 1) * 64); glds3(wqb + (size_t)((KS(s) * 64 + 8 * w) * 3072), v_ + g * 128, v_ + gk * 128, v_ + gv_ * 128, lds0 + WR_OFF + s * WSLOT + w * 3072);
                              glds1(x + (size_t)(64 * i + 8 * w) * 1024 + KS(s) * 128, xv0 ^ ((w & 1) * 64), lds0 + X_OFF + s * XKS + (16 + 8 * w) * 128); };
    first(0);
    { const char* pc = (const char*)__builtin_amdgcn_s_getpc();
      touch(pc - ((uintptr_t)pc & 127), ((idx * 8 + w) * 4 + (lane & 3)) % (PFKB * 8) * 128, lds0 + DROP_OFF); }
    first(1);
    touch(wqa + blockIdx.x * 6144, (w * 6 + (lane < 6 ? lane : 5)) * 128, lds0 + DROP_OFF);
#define SETW(n, v) do { if (lane == 0) __hip_atomic_store((LAS int*)(lds + PROG_OFF) + (n), (v), __ATOMIC_RELAXED, __HIP_MEMORY_SCOPE_WORKGROUP); } while (0)
#define WAITW(n, v) do { while (!__all(__hip_atomic_load((LAS int*)(lds + PROG_OFF) + (n) + (lane & 3), __ATOMIC_RELAXED, __HIP_MEMORY_SCOPE_WORKGROUP) >= (v))) __builtin_amdgcn_s_sleep(1); asm volatile("" ::: "memory"); } while (0)
    const char* kvsrc = ws + WS_KV + h * 131072 + wq * 8192;
#define KVLD(b) do { glds4c(kvsrc + (b) * 32768, lane16, lds0 + (b) * 32768 + wq * 8192); glds4c(kvsrc + (b) * 32768 + 4096, lane16, lds0 + (b) * 32768 + wq * 8192 + 4096); } while (0)
    if (w >= 4) {
        __builtin_amdgcn_s_setprio(3);
        const unsigned wv0 = wvl + 16 * wq * 3072, wv1 = (wv0 ^ 64) + 8 * 3072;
        const unsigned wdst = lds0 + WR_OFF + wq * 6144;
        auto issue = [&](auto S) { constexpr int s = S.v;
            if constexpr (s == 8 || s == 9) {
                const int st = 4 * (s - 8) + wq; const char* hs = x + (size_t)(i ? 64 * i - 16 : 0) * 1024 + KS(st) * 128;
                glds1(hs, xv0, lds0 + X_OFF + st * XKS); glds1(hs, xv1, lds0 + X_OFF + st * XKS + 1024); }
            const char* wsrc = (s < 8 ? wqb : wqa) + (size_t)(KS(s) * 64 * 3072);
            const int c1 = s < 8 ? gk : g, c2 = s < 8 ? gv_ : g;
            glds3(wsrc, wv0 + g * 128, wv0 + c1 * 128, wv0 + c2 * 128, wdst + (s % 3) * WSLOT); glds3(wsrc, wv1 + g * 128, wv1 + c1 * 128, wv1 + c2 * 128, wdst + (s % 3) * WSLOT + 3072); };
        wait_bar<5 + TCH>();
        if (w == 4) glds1(gw, lane16, lds0 + GW_OFF);
        if (w == 5) { const void* sbase = ws + WS_EPOCH; const unsigned voff = (lane & 1) * 4, ldsb = lds0 + EP_OFF; unsigned sv; asm volatile(GLDS_OPEN "global_load_lds_dword %1, %2 sc1\n\ts_mov_b32 m0, %0" GLDS_ARGS); }
        issue(IC<2>{});
        f32x4 ga = {};
        const lds_cptr gwrd = L + GW_OFF + lg * 16;
        static_for<0, 14>([&](auto S) { constexpr int s = S.v;
            constexpr int cnt[14] = {6, 6, 6, 6, 6, 6, 8, 63, 6, 6, 6, 6, 6, 6};
            wait_bar<cnt[s]>();
            u32x4 g0, g1, g2, g3;
            if constexpr (s >= 4 && s < 12) if (g == (i & 7)) { constexpr int c = s - 4;
                g0 = lds128(gwrd + KS(c) * 128); g1 = lds128(xrd0 + c * XKS + (1 + wq) * 2048); g2 = lds128(gwrd + KS(c) * 128 + 64); g3 = lds128(xrd1 + c * XKS + (1 + wq) * 2048); }
            if constexpr (s + 3 < 16) issue(IC<s + 3>{});
            if constexpr (s == 7) tag = mktag(*(LAS const unsigned long long*)(lds + EP_OFF));
            if constexpr (s >= 4 && s < 12) if (g == (i & 7)) { ga = mfma16(g0, g1, ga); ga = mfma16(g2, g3, ga); }
            if constexpr (s == 7) wait_bar<14>();
        });
        VM0(); SETW(4 + wq, 1);
        { gu64* f0 = (gu64*)(ws + WS_KVFLAG) + (((h - 4 * (lane < 32 ? GKS : GVS)) & 31) * 32 + (lane & 31)) * FS; gu64* f1 = (gu64*)(ws + WS_KVFLAG) + (h * 32 + 4 * j + wq) * FS;
#pragma unroll 1
          for (unsigned spins = 0; spins < kSpinCap; ++spins) {
              const unsigned long long a = __hip_atomic_load(f0, RLX_AGENT), b = __hip_atomic_load(f1, RLX_AGENT);
              if (__all(a == tag && b == tag)) break;
              __builtin_amdgcn_s_sleep(1); }
          __builtin_amdgcn_fence(__ATOMIC_ACQUIRE, "wavefront"); }
        KVLD(0);
        WAITW(0, 1);
        glds2c(ws + WS_Q + (h * 64 + 8 * j + 2 * wq) * 1024, lane16, lds0 + SPARE_OFF + wq * 2048); KVLD(1);
        wait_bar<0>();
        if (g == (i & 7) && lane < 16) __hip_atomic_store((gu64*)(ws + WS_GATE) + 64 * i + 16 * wq + lane, (tag & 0xffffffff00000000ull) | __float_as_uint(1.f / (1.f + __expf(-ga[0]))), RLX_AGENT);
    } else {
        const char* xsrc = x + (size_t)(64 * i + 16 * w) * 1024; const unsigned xdst = lds0 + X_OFF + (16 + 16 * w) * 128;
#define XLD(s) do { glds1(xsrc + KS(s) * 128, xv0, xdst + (s) * XKS); glds1(xsrc + KS(s) * 128, xv1, xdst + (s) * XKS + 1024); } while (0)
        { const unsigned ov = (l15 == 0 || l15 == 4) ? 0x3F803F80u : 0u; *(LAS u32x4*)(lds + ONES_OFF + tid * 16) = (u32x4){ov, ov, ov, ov}; }
        if (tid < 24) *(LAS int*)(lds + PROG_OFF + tid * 4) = 0;
        const lds_cptr wrd = L + WR_OFF + lg * 3072 + ((lane >> 2) & 3) * 128 + ((w ^ (((lane >> 3) & 1) | ((lg & 1) << 1))) * 32) + (lane & 3) * 8;
        auto readf = [&](auto S, Frags& f) { constexpr int s = S.v;
#pragma unroll
            for (int cc = 0; cc < 2; ++cc) {
#pragma unroll
                for (int sec = 0; sec < 3; ++sec) f.w[cc][sec] = tr128(wrd + (s % 3) * WSLOT + cc * 12288 + sec * 1024);
#pragma unroll
                for (int t = (s < 8 ? 1 : 0); t < 5; ++t) f.x[cc][t] = lds128((cc ? xrd1 : xrd0) + (s & 7) * XKS + t * 2048);
            } };
        f32x4 q[5] = {}, k[5] = {}, v[5] = {};
        Frags fr[2];
        wait_bar<5 + TCH>();
        XLD(2); XLD(3);
        readf(IC<0>{}, fr[0]);
        auto mm = [&](auto S, const Frags& f) { constexpr int s = S.v;
#pragma unroll
            for (int cc = 0; cc < 2; ++cc)
#pragma unroll
                for (int t = (s < 8 ? 1 : 0); t < 5; ++t) { k[t] = mfma16(f.w[cc][1], f.x[cc][t], k[t]); v[t] = mfma16(f.x[cc][t], f.w[cc][2], v[t]); if (t) q[t] = mfma16(f.w[cc][0], f.x[cc][t], q[t]); } };
        static_for<0, 16>([&](auto S) { constexpr int s = S.v;
            constexpr int cnt[14] = {5, 4, 4, 4, 4, 2, 0, 63, 63, 63, 63, 63, 63, 63};
            if constexpr (s == 7) {
                BAR(); SBAR();
                mm(S, fr[1]);
                const unsigned dst = WS_KV + w * 131072 + (i >> 1) * 8192 + (i & 1) * 2048;
#pragma unroll
                for (int t = 0; t < 2; ++t) {
                    STC(join16(pack4(k[1 + 2 * t]), pack4(k[2 + 2 * t])), dst + gk * 524288 + t * 1024);
                    STC(join16(pack4(v[1 + 2 * t]), pack4(v[2 + 2 * t])), dst + gv_ * 524288 + 4096 + t * 1024);
                    STC(join16(pack4(q[1 + 2 * t] * QSCALE), pack4(q[2 + 2 * t] * QSCALE)), WS_Q + ((4 * g + w) * 64 + 2 * i + t) * 1024); }
#pragma unroll
                for (int t = 0; t < 5; ++t) q[t] = k[t] = v[t] = (f32x4){};
                SBAR();
            }
            if constexpr (s < 14) { wait_bar<cnt[s]>(); }
            else if constexpr (s == 14) { asm volatile("s_waitcnt lgkmcnt(0)" ::: "memory"); SETW(w, 1); WAITW(4, 1); }
            SBAR();
            if constexpr (s < 15) readf(IC<s + 1>{}, fr[(s + 1) & 1]);
            if constexpr (s != 7) mm(S, fr[s & 1]);
            if constexpr (s < 15 && s != 7) {
#pragma unroll
                for (int u = 0; u < 22; ++u) { __builtin_amdgcn_sched_group_barrier(0x8, 1, 0); __builtin_amdgcn_sched_group_barrier(0x100, 1, 0); }
                __builtin_amdgcn_sched_group_barrier(0x8, 8, 0); }
            SBAR();
            if constexpr (s < 4) XLD(s + 4);
            if constexpr (s == 4) tag = mktag(*(LAS const unsigned long long*)(lds + EP_OFF));
            if constexpr (s == 9) { VM0(); if (lane == 0) __hip_atomic_store((gu64*)(ws + WS_KVFLAG) + ((4 * g + w) * 32 + i) * FS, tag, RLX_AGENT); }
        });
        u32x2 kf[5], vf[5], of[4];
        f32x4 mc, mp, mp0;
#pragma unroll
        for (int r = 0; r < 4; ++r) { const int dc = l15 - (4 * lg + r); mc[r] = (dc >= 0 && !(dc & 1)) ? 0.f : -3e38f; mp[r] = (dc <= -2 && !(dc & 1)) ? 0.f : -3e38f; mp0[r] = i ? mp[r] : -3e38f; }
#pragma unroll
        for (int t = 0; t < 5; ++t) { kf[t] = pack4(k[t]); vf[t] = pack4(v[t]); }
        const u32x2 one2 = {0x3F803F80u, 0x3F803F80u};
#pragma unroll
        for (int sb = 0; sb < 4; ++sb) {
            const f32x4 z = {}; const u32x2 qs = pack4(q[sb + 1] * QSCALE);
            f32x4 sp = mfma16k16(kf[sb], qs, sb ? mp : mp0), sc = mfma16k16(kf[sb + 1], qs, mc);
            float m = mx(mx(mx(mx(sc[0], sc[1]), sc[2]), mx(mx(sc[3], sp[0]), sp[1])), mx(sp[2], sp[3]));
            m = xmax32(xmax16(m));
#pragma unroll
            for (int r = 0; r < 4; ++r) { sc[r] = __builtin_amdgcn_exp2f(sc[r] - m); sp[r] = __builtin_amdgcn_exp2f(sp[r] - m); }
            const u32x2 pp = pack4(sp), pc = pack4(sc);
            f32x4 l = mfma16k16(one2, pp, z); l = mfma16k16(one2, pc, l);
            f32x4 o = mfma16k16(vf[sb], pp, z); o = mfma16k16(vf[sb + 1], pc, o);
            o *= __builtin_amdgcn_rcpf(l[0]);
            of[sb] = pack4(o);
        }
        STC(join16(of[0], of[1]), WS_OA + (2 * i * 32 + 4 * g + w) * 1024);
        STC(join16(of[2], of[3]), WS_OA + ((2 * i + 1) * 32 + 4 * g + w) * 1024);
        BAR();
        __builtin_amdgcn_s_setprio(3);
        KVLD(2); KVLD(3);
    }
    const u32x4 qa = lds128(L + SPARE_OFF + wq * 2048 + lane16), qb = lds128(L + SPARE_OFF + wq * 2048 + 1024 + lane16);
    const int tt = w & 1, kq = w >> 1, kh = w >> 2;
    const unsigned osrc = ((2 * i3 + tt) * 32 + 8 * kq) * 1024;
    const unsigned ovoff = (lane >> 3) * 1024 + g3 * 128 + (((lane & 7) ^ (((lane >> 4) & 1) << 2)) * 16);
    u32x4 of[8];
    unsigned long long gg, fb;
    gu64* obf = (gu64*)(ws + WS_OBFLAG) + ((2 * i3 + tt) * 32 + 8 * kq) * FS;
#define POLLB() asm volatile("s_nop 4\n\tglobal_load_dwordx2 %0, %1, %2 sc1" : "=v"(fb) : "v"((lane & 7) * 8 * FS), "s"(obf) : "memory")
    float gv;
    const lds_cptr wo0 = L + kq * 16384 + (8 * (lane >> 5) + ((lane >> 2) & 3)) * 128 + (((lane >> 3) & 1) * 64) + (lg & 1) * 32 + (lane & 3) * 8;
    const lds_cptr wo1 = L + kq * 16384 + (8 * (lane >> 5) + ((lane >> 2) & 3)) * 128 + ((1 ^ ((lane >> 3) & 1)) * 64) + (lg & 1) * 32 + (lane & 3) * 8;
    f32x16 a0 = {}, a1 = {};
    auto proj_a = [&]() {
#pragma unroll
            for (int u = 0; u < 8; ++u) { a0 = mfma32(tr128(wo0 + u * 2048), of[u], a0); a1 = mfma32(tr128(wo1 + u * 2048), of[u], a1); }
            a0 *= gv; a1 *= gv; PIN(a0); PIN(a1); };
    const int sidx = (int)0xDDDDDDDDu;
    const bool real = (lane & 16) == 0;
    auto finish = [&](f32x16 o, const u32x4 qf, int T) {
        float chk = o[8];
#pragma unroll
        for (int r = 0; r < 8; ++r) chk = mx(chk, fabsf(o[r]) * 0x1p-30f);
        if (__builtin_expect(__any(!(chk < 0x1p90f)), 0)) {
            const f32x16 zero = {}; float m = -3e38f;
            const unsigned ksrc = WS_KV + h * 131072;
#pragma unroll 1
            for (int t = 0; t < 64; ++t) { f32x16 s = mfma32(LDC(ksrc + (t >> 2) * 8192 + (t & 3) * 1024), qf, zero);
#pragma unroll
                for (int r = 0; r < 16; ++r) m = fmaxf(m, s[r]); }
            m = xmax32(m); o = zero;
            const unsigned vl = (lane >> 5) * 512 + l15 * 16;
            const u32x4 ones = {0x3F803F80u, 0x3F803F80u, 0x3F803F80u, 0x3F803F80u};
#pragma unroll 1
            for (int t = 0; t < 64; ++t) { const unsigned ts = ksrc + (t >> 2) * 8192 + (t & 3) * 1024; f32x16 s = mfma32(LDC(ts), qf, zero);
                const u32x4 a = __builtin_amdgcn_raw_buffer_load_b128(wsr, vl, ts + 4096, 16), c = __builtin_amdgcn_raw_buffer_load_b128(wsr, vl + 256, ts + 4096, 16);
#pragma unroll
                for (int r = 0; r < 16; ++r) s[r] = __builtin_amdgcn_exp2f(s[r] - m);
                o = smf(real ? a : ones, lo8(s), o, sidx); o = smf(real ? c : ones, hi8(s), o, sidx); }
        }
        const float rl = __builtin_amdgcn_rcpf(o[8]);
        STC(join32(cvtpk(o[0] * rl, o[1] * rl), cvtpk(o[2] * rl, o[3] * rl), cvtpk(o[4] * rl, o[5] * rl), cvtpk(o[6] * rl, o[7] * rl)), WS_OB + (T * 32 + h) * 1024);
    };
    {
        unsigned kba = lds0 + kh * 8192 + lane16, vba = lds0 + (real ? kh * 8192 + 4096 : ONES_OFF) + (lane >> 5) * 512 + l15 * 16;
        const int vstep = real ? 16384 : 0;
        f32x16 sa, sb, ca, cb, oa = {}, ob = {};
        u32x4 kf = lds128(L + lane16), va = lds128((lds_cptr)(uintptr_t)vba), vc = va;
        { const f32x16 zero = {}; sa = mfma32(kf, qa, zero); sb = mfma32(kf, qb, zero); kf = lds128((lds_cptr)(uintptr_t)kba);
          float ma = sa[0], mb = sb[0];
#pragma unroll
          for (int r = 1; r < 16; ++r) { ma = mx(ma, sa[r]); mb = mx(mb, sb[r]); }
          ma = xmax32(ma); mb = xmax32(mb);
#pragma unroll
          for (int r = 0; r < 16; ++r) { ca[r] = -ma; cb[r] = -mb; sb[r] = 0.f; } }
        PIN(ca); PIN(cb);
        if (w >= 4) SETW(w, 2);
        sa = mfma32(kf, qa, ca);
        auto run = [&](int n) {
            asm volatile("s_waitcnt lgkmcnt(0)\n\ts_nop 11\n"
            "1:\n\t"
            "v_exp_f32 v32, v32\n\tv_exp_f32 v33, v33\n\tv_smfmac_f32_32x32x32_bf16 %[ob], %[vc], v[56:63], %[ix]\n\tds_read_b128 %[vc], %[vb] offset:256\n\tv_exp_f32 v34, v34\n\tv_exp_f32 v35, v35\n\tv_exp_f32 v36, v36\n\tv_exp_f32 v37, v37\n\tv_mfma_f32_32x32x16_bf16 v[48:63], %[kf], %[qb], %[cb]\n\tds_read_b128 %[kf], %[kb] offset:1024\n\tv_exp_f32 v38, v38\n\tv_exp_f32 v39, v39\n\tv_exp_f32 v40, v40\n\tv_exp_f32 v41, v41\n\ts_waitcnt lgkmcnt(2)\n\tv_smfmac_f32_32x32x32_bf16 %[oa], %[va], v[32:39], %[ix]\n\tv_exp_f32 v42, v42\n\tv_exp_f32 v43, v43\n\tv_exp_f32 v44, v44\n\tv_exp_f32 v45, v45\n\tv_exp_f32 v46, v46\n\tv_exp_f32 v47, v47\n\t"
            "v_exp_f32 v48, v48\n\tv_exp_f32 v49, v49\n\ts_waitcnt lgkmcnt(0)\n\tv_smfmac_f32_32x32x32_bf16 %[oa], %[vc], v[40:47], %[ix]\n\tv_exp_f32 v50, v50\n\tv_exp_f32 v51, v51\n\tv_exp_f32 v52, v52\n\tv_exp_f32 v53, v53\n\tv_mfma_f32_32x32x16_bf16 v[32:47], %[kf], %[qa], %[ca]\n\tv_exp_f32 v54, v54\n\tv_exp_f32 v55, v55\n\tv_exp_f32 v56, v56\n\tv_exp_f32 v57, v57\n\tv_smfmac_f32_32x32x32_bf16 %[ob], %[va], v[48:55], %[ix]\n\tds_read_b128 %[va], %[vb] offset:1024\n\tv_exp_f32 v58, v58\n\tv_exp_f32 v59, v59\n\tv_exp_f32 v60, v60\n\tv_exp_f32 v61, v61\n\tv_exp_f32 v62, v62\n\tv_exp_f32 v63, v63\n\t"
            "v_exp_f32 v32, v32\n\tv_exp_f32 v33, v33\n\tv_smfmac_f32_32x32x32_bf16 %[ob], %[vc], v[56:63], %[ix]\n\tds_read_b128 %[vc], %[vb] offset:1280\n\tv_exp_f32 v34, v34\n\tv_exp_f32 v35, v35\n\tv_exp_f32 v36, v36\n\tv_exp_f32 v37, v37\n\tv_mfma_f32_32x32x16_bf16 v[48:63], %[kf], %[qb], %[cb]\n\tds_read_b128 %[kf], %[kb] offset:2048\n\tv_exp_f32 v38, v38\n\tv_exp_f32 v39, v39\n\tv_exp_f32 v40, v40\n\tv_exp_f32 v41, v41\n\ts_waitcnt lgkmcnt(2)\n\tv_smfmac_f32_32x32x32_bf16 %[oa], %[va], v[32:39], %[ix]\n\tv_exp_f32 v42, v42\n\tv_exp_f32 v43, v43\n\tv_exp_f32 v44, v44\n\tv_exp_f32 v45, v45\n\tv_exp_f32 v46, v46\n\tv_exp_f32 v47, v47\n\t"
            "v_exp_f32 v48, v48\n\tv_exp_f32 v49, v49\n\ts_waitcnt lgkmcnt(0)\n\tv_smfmac_f32_32x32x32_bf16 %[oa], %[vc], v[40:47], %[ix]\n\tv_exp_f32 v50, v50\n\tv_exp_f32 v51, v51\n\tv_exp_f32 v52, v52\n\tv_exp_f32 v53, v53\n\tv_mfma_f32_32x32x16_bf16 v[32:47], %[kf], %[qa], %[ca]\n\tv_exp_f32 v54, v54\n\tv_exp_f32 v55, v55\n\tv_exp_f32 v56, v56\n\tv_exp_f32 v57, v57\n\tv_smfmac_f32_32x32x32_bf16 %[ob], %[va], v[48:55], %[ix]\n\tds_read_b128 %[va], %[vb] offset:2048\n\tv_exp_f32 v58, v58\n\tv_exp_f32 v59, v59\n\tv_exp_f32 v60, v60\n\tv_exp_f32 v61, v61\n\tv_exp_f32 v62, v62\n\tv_exp_f32 v63, v63\n\t"
            "v_exp_f32 v32, v32\n\tv_exp_f32 v33, v33\n\tv_smfmac_f32_32x32x32_bf16 %[ob], %[vc], v[56:63], %[ix]\n\tds_read_b128 %[vc], %[vb] offset:2304\n\tv_exp_f32 v34, v34\n\tv_exp_f32 v35, v35\n\tv_exp_f32 v36, v36\n\tv_exp_f32 v37, v37\n\tv_mfma_f32_32x32x16_bf16 v[48:63], %[kf], %[qb], %[cb]\n\tds_read_b128 %[kf], %[kb] offset:3072\n\tv_exp_f32 v38, v38\n\tv_exp_f32 v39, v39\n\tv_exp_f32 v40, v40\n\tv_exp_f32 v41, v41\n\ts_waitcnt lgkmcnt(2)\n\tv_smfmac_f32_32x32x32_bf16 %[oa], %[va], v[32:39], %[ix]\n\tv_exp_f32 v42, v42\n\tv_exp_f32 v43, v43\n\tv_exp_f32 v44, v44\n\tv_exp_f32 v45, v45\n\tv_exp_f32 v46, v46\n\tv_exp_f32 v47, v47\n\t"
            "v_exp_f32 v48, v48\n\tv_exp_f32 v49, v49\n\ts_waitcnt lgkmcnt(0)\n\tv_smfmac_f32_32x32x32_bf16 %[oa], %[vc], v[40:47], %[ix]\n\tv_exp_f32 v50, v50\n\tv_exp_f32 v51, v51\n\tv_exp_f32 v52, v52\n\tv_exp_f32 v53, v53\n\tv_mfma_f32_32x32x16_bf16 v[32:47], %[kf], %[qa], %[ca]\n\tv_exp_f32 v54, v54\n\tv_exp_f32 v55, v55\n\tv_exp_f32 v56, v56\n\tv_exp_f32 v57, v57\n\tv_smfmac_f32_32x32x32_bf16 %[ob], %[va], v[48:55], %[ix]\n\tds_read_b128 %[va], %[vb] offset:3072\n\tv_exp_f32 v58, v58\n\tv_exp_f32 v59, v59\n\tv_exp_f32 v60, v60\n\tv_exp_f32 v61, v61\n\tv_exp_f32 v62, v62\n\tv_exp_f32 v63, v63\n\t"
            "v_exp_f32 v32, v32\n\tv_exp_f32 v33, v33\n\tv_smfmac_f32_32x32x32_bf16 %[ob], %[vc], v[56:63], %[ix]\n\tds_read_b128 %[vc], %[vb] offset:3328\n\tv_exp_f32 v34, v34\n\tv_exp_f32 v35, v35\n\tv_exp_f32 v36, v36\n\tv_exp_f32 v37, v37\n\tv_mfma_f32_32x32x16_bf16 v[48:63], %[kf], %[qb], %[cb]\n\tds_read_b128 %[kf], %[kb] offset:16384\n\tv_exp_f32 v38, v38\n\tv_exp_f32 v39, v39\n\tv_exp_f32 v40, v40\n\tv_exp_f32 v41, v41\n\ts_waitcnt lgkmcnt(2)\n\tv_smfmac_f32_32x32x32_bf16 %[oa], %[va], v[32:39], %[ix]\n\tv_exp_f32 v42, v42\n\tv_exp_f32 v43, v43\n\tv_exp_f32 v44, v44\n\tv_exp_f32 v45, v45\n\tv_exp_f32 v46, v46\n\tv_exp_f32 v47, v47\n\t"
            "v_exp_f32 v48, v48\n\tv_exp_f32 v49, v49\n\ts_waitcnt lgkmcnt(0)\n\tv_smfmac_f32_32x32x32_bf16 %[oa], %[vc], v[40:47], %[ix]\n\tv_exp_f32 v50, v50\n\tv_exp_f32 v51, v51\n\tv_exp_f32 v52, v52\n\tv_exp_f32 v53, v53\n\tv_mfma_f32_32x32x16_bf16 v[32:47], %[kf], %[qa], %[ca]\n\tv_exp_f32 v54, v54\n\tv_exp_f32 v55, v55\n\tv_exp_f32 v56, v56\n\tv_exp_f32 v57, v57\n\tv_add_u32 %[vb], %[vb], %[vs]\n\tv_smfmac_f32_32x32x32_bf16 %[ob], %[va], v[48:55], %[ix]\n\tds_read_b128 %[va], %[vb] offset:0\n\tv_add_u32 %[kb], 0x4000, %[kb]\n\ts_sub_u32 %[n], %[n], 1\n\tv_exp_f32 v58, v58\n\tv_exp_f32 v59, v59\n\tv_exp_f32 v60, v60\n\tv_exp_f32 v61, v61\n\tv_exp_f32 v62, v62\n\tv_exp_f32 v63, v63\n\t"
            "s_cmp_lg_u32 %[n], 0\n\ts_cbranch_scc1 1b\n\ts_waitcnt lgkmcnt(0)\n\ts_nop 7"
            : "+{v[32:47]}"(sa), "+{v[48:63]}"(sb), [oa] "+v"(oa), [ob] "+v"(ob), [kf] "+v"(kf), [va] "+v"(va), [vc] "+v"(vc), [kb] "+v"(kba), [vb] "+v"(vba), [n] "+s"(n)
            : [qa] "v"(qa), [qb] "v"(qb), [ca] "v"(ca), [cb] "v"(cb), [vs] "v"(vstep), [ix] "v"(sidx) : "scc", "memory");
        };
        run(2);
        __builtin_amdgcn_s_setprio(2);
        run(2);
        if (w < 4) { VM0(); if (lane == 0) __hip_atomic_store((gu64*)(ws + WS_OAFLAG) + (i * 32 + 4 * g + w) * FS, tag, RLX_AGENT); }
        SETW(8 + w, 1);
        WAITW(8, 1);
        kf = lds128((lds_cptr)(uintptr_t)kba); va = lds128((lds_cptr)(uintptr_t)vba); sa = mfma32(kf, qa, ca);
        if (w >= 4) WAITW(12, 1); else WAITW(4, 2);
#pragma unroll
        for (int u = 0; u < 8; ++u) glds1(woa + (size_t)(64 * (2 * wq + kh) + 8 * u) * 1024, ovoff, lds0 + (2 * wq + kh) * 8192 + u * 1024);
        if (w < 2) touch(wob + g3 * 128, (128 * (idx & 3) + 64 * w + lane) * 1024, lds0 + DROP_OFF);
        __builtin_amdgcn_s_setprio(1);
        run(1);
#pragma unroll 1
        for (unsigned spins = 0; spins < kSpinCap; ++spins) {
            const unsigned long long f = __hip_atomic_load((gu64*)(ws + WS_OAFLAG) + (i3 * 32 + 8 * kq + (lane & 7)) * FS, RLX_AGENT);
            gg = __hip_atomic_load((gu64*)(ws + WS_GATE) + 64 * i3 + 32 * tt + (lane & 31), RLX_AGENT);
            if (__all(f == tag && (gg >> 32) == (tag >> 32))) break;
            __builtin_amdgcn_s_sleep(1); }
        VM0();
#pragma unroll
        for (int u = 0; u < 8; ++u) of[u] = LDC(WS_OA + osrc + u * 1024);
        SETW(8 + w, 2);
        __builtin_amdgcn_s_setprio(0);
        run(3);
#pragma unroll
        for (int u = 0; u < 8; ++u) PIN(of[u]);
        if (w >= 4) POLLB();
        ob = smf(vc, hi8(sb), ob, sidx);
        f32x16 give, o;
#pragma unroll
        for (int r = 0; r < 16; ++r) { give[r] = r < 9 ? (kh ? oa[r] : ob[r]) : 0.f; o[r] = r < 9 ? (kh ? ob[r] : oa[r]) : 0.f; }
        LAS char* xw = (LAS char*)lds + SPARE_OFF + w * 3072 + lane16;
        *(LAS f32x4*)xw = __builtin_shufflevector(give, give, 0, 1, 2, 3); *(LAS f32x4*)(xw + 1024) = __builtin_shufflevector(give, give, 4, 5, 6, 7); *(LAS float*)(xw + 2048) = give[8];
        gv = __uint_as_float((unsigned)gg);
        if (w < 4) {
            WAITW(8, 2); WAITW(12, 2);
            proj_a();
            SETW(8 + w, 3); WAITW(8, 3);
#pragma unroll
            for (int u = 0; u < 16; ++u) glds1(wob + (size_t)(128 * w + 8 * u) * 1024, ovoff, lds0 + (w < 2 ? w * 16384 + u * 1024 : w * 32768 + (u >> 3) * 16384 + (u & 7) * 1024));
            VM0(); POLLB(); }
        BAR();
        LAS const char* xr = L + SPARE_OFF + (w ^ 4) * 3072 + lane16;
        const f32x4 r0 = *(LAS const f32x4*)xr, r1 = *(LAS const f32x4*)(xr + 1024);
#pragma unroll
        for (int r = 0; r < 4; ++r) { o[r] += r0[r]; o[4 + r] += r1[r]; }
        o[8] += *(LAS const float*)(xr + 2048);
        finish(o, kh ? qb : qa, 8 * j + 2 * wq + kh);
    }
    {
        f32x16 b0 = {}, b1 = {};
        u32x4 wf[16], ob8[8];
        asm volatile("s_waitcnt vmcnt(0)" : "+v"(fb) :: "memory");
        if (lane == 0) __hip_atomic_store((gu64*)(ws + WS_OBFLAG) + ((8 * j + 2 * wq + kh) * 32 + h) * FS, tag, RLX_AGENT);
        unsigned got = (unsigned)__builtin_amdgcn_ballot_w64(fb == tag) & 0xffu;
        const char* pb = ws + WS_OB + osrc + 4096;
        asm volatile("s_nop 4\n\t"
            "s_bitcmp1_b32 %[got], 0\n\ts_cbranch_scc0 10f\n\tglobal_load_dwordx4 %0, %[vo], %[pb] offset:-4096 sc1\n10:\n\t"
            "s_bitcmp1_b32 %[got], 1\n\ts_cbranch_scc0 11f\n\tglobal_load_dwordx4 %1, %[vo], %[pb] offset:-3072 sc1\n11:\n\t"
            "s_bitcmp1_b32 %[got], 2\n\ts_cbranch_scc0 12f\n\tglobal_load_dwordx4 %2, %[vo], %[pb] offset:-2048 sc1\n12:\n\t"
            "s_bitcmp1_b32 %[got], 3\n\ts_cbranch_scc0 13f\n\tglobal_load_dwordx4 %3, %[vo], %[pb] offset:-1024 sc1\n13:\n\t"
            "s_bitcmp1_b32 %[got], 4\n\ts_cbranch_scc0 14f\n\tglobal_load_dwordx4 %4, %[vo], %[pb] offset:0 sc1\n14:\n\t"
            "s_bitcmp1_b32 %[got], 5\n\ts_cbranch_scc0 15f\n\tglobal_load_dwordx4 %5, %[vo], %[pb] offset:1024 sc1\n15:\n\t"
            "s_bitcmp1_b32 %[got], 6\n\ts_cbranch_scc0 16f\n\tglobal_load_dwordx4 %6, %[vo], %[pb] offset:2048 sc1\n16:\n\t"
            "s_bitcmp1_b32 %[got], 7\n\ts_cbranch_scc0 17f\n\tglobal_load_dwordx4 %7, %[vo], %[pb] offset:3072 sc1\n17:\n\t"
            : "=&v"(ob8[0]), "=&v"(ob8[1]), "=&v"(ob8[2]), "=&v"(ob8[3]), "=&v"(ob8[4]), "=&v"(ob8[5]), "=&v"(ob8[6]), "=&v"(ob8[7])
            : [got] "s"(got), [pb] "s"(pb), [vo] "v"(lane16) : "scc", "memory");
        if (w >= 4) { proj_a(); SETW(8 + w, 3); }
        const int wb0 = kq < 2 ? 0 : kq * 16384, wb1 = kq < 2 ? 0 : kq * 16384 + 8192;
#pragma unroll
        for (int u = 0; u < 8; ++u) { wf[2 * u] = tr128(wo0 + (u < 4 ? wb0 : wb1) + u * 2048); wf[2 * u + 1] = tr128(wo1 + (u < 4 ? wb0 : wb1) + u * 2048); }
        { unsigned fresh, spins = kSpinCap;
          asm volatile("s_nop 4\n\t"
            "s_cmp_eq_u32 %[got], 0xff\n\ts_cbranch_scc1 3f\n2:\n\t"
            "global_load_dwordx2 %[fb], %[fo], %[fp] sc1\n\ts_waitcnt vmcnt(0)\n\t"
            "v_cmp_eq_u64 vcc, %[tg], %[fb]\n\ts_nop 1\n\ts_and_b32 %[fr], vcc_lo, 0xff\n\ts_andn2_b32 %[fr], %[fr], %[got]\n\t"
            "s_bitcmp1_b32 %[fr], 0\n\ts_cbranch_scc0 10f\n\tglobal_load_dwordx4 %0, %[vo], %[pb] offset:-4096 sc1\n10:\n\t"
            "s_bitcmp1_b32 %[fr], 1\n\ts_cbranch_scc0 11f\n\tglobal_load_dwordx4 %1, %[vo], %[pb] offset:-3072 sc1\n11:\n\t"
            "s_bitcmp1_b32 %[fr], 2\n\ts_cbranch_scc0 12f\n\tglobal_load_dwordx4 %2, %[vo], %[pb] offset:-2048 sc1\n12:\n\t"
            "s_bitcmp1_b32 %[fr], 3\n\ts_cbranch_scc0 13f\n\tglobal_load_dwordx4 %3, %[vo], %[pb] offset:-1024 sc1\n13:\n\t"
            "s_bitcmp1_b32 %[fr], 4\n\ts_cbranch_scc0 14f\n\tglobal_load_dwordx4 %4, %[vo], %[pb] offset:0 sc1\n14:\n\t"
            "s_bitcmp1_b32 %[fr], 5\n\ts_cbranch_scc0 15f\n\tglobal_load_dwordx4 %5, %[vo], %[pb] offset:1024 sc1\n15:\n\t"
            "s_bitcmp1_b32 %[fr], 6\n\ts_cbranch_scc0 16f\n\tglobal_load_dwordx4 %6, %[vo], %[pb] offset:2048 sc1\n16:\n\t"
            "s_bitcmp1_b32 %[fr], 7\n\ts_cbranch_scc0 17f\n\tglobal_load_dwordx4 %7, %[vo], %[pb] offset:3072 sc1\n17:\n\t"
            "s_or_b32 %[got], %[got], %[fr]\n\ts_cmp_eq_u32 %[got], 0xff\n\ts_cbranch_scc1 3f\n\ts_sleep 1\n\ts_sub_u32 %[sp], %[sp], 1\n\ts_cbranch_scc0 2b\n"
            "3:\n\ts_waitcnt vmcnt(0)"
            : "+v"(ob8[0]), "+v"(ob8[1]), "+v"(ob8[2]), "+v"(ob8[3]), "+v"(ob8[4]), "+v"(ob8[5]), "+v"(ob8[6]), "+v"(ob8[7]), [got] "+s"(got), [fr] "=&s"(fresh), [sp] "+s"(spins), [fb] "=&v"(fb)
            : [pb] "s"(pb), [vo] "v"(lane16), [fo] "v"((lane & 7) * 8 * FS), [fp] "s"(obf), [tg] "s"(tag) : "scc", "vcc", "memory"); }
#pragma unroll
        for (int u = 0; u < 8; ++u) { b0 = mfma32(wf[2 * u], ob8[u], b0); b1 = mfma32(wf[2 * u + 1], ob8[u], b1); }
        a0 += b0 * (1.f - gv); a1 += b1 * (1.f - gv);
        WAITW(12, 3);
        const int cs = tt ? 1024 : 512;
        LAS f32x4* rw = (LAS f32x4*)(lds + (tt ? 73728 : 32768) + kq * 2048 + lane16);
        if (kq != 0) { rw[0] = __builtin_shufflevector(a0, a0, 0, 1, 2, 3); rw[64] = __builtin_shufflevector(a0, a0, 4, 5, 6, 7); }
        if (kq != 1) { rw[cs] = __builtin_shufflevector(a0, a0, 8, 9, 10, 11); rw[cs + 64] = __builtin_shufflevector(a0, a0, 12, 13, 14, 15); }
        if (kq != 2) { rw[2 * cs] = __builtin_shufflevector(a1, a1, 0, 1, 2, 3); rw[2 * cs + 64] = __builtin_shufflevector(a1, a1, 4, 5, 6, 7); }
        if (kq != 3) { rw[3 * cs] = __builtin_shufflevector(a1, a1, 8, 9, 10, 11); rw[3 * cs + 64] = __builtin_shufflevector(a1, a1, 12, 13, 14, 15); }
        const f32x16 am = kq < 2 ? a0 : a1; const f32x8 own = (kq & 1) ? hi8(am) : lo8(am);
        BAR();
        if (blockIdx.x == 0 && tid == 0) __hip_atomic_store((gu64*)(ws + WS_EPOCH), tag, RLX_AGENT);
        LAS const f32x4* rr = (LAS const f32x4*)(lds + (tt ? 73728 : 32768) + kq * cs * 16 + lane16);
        const int s1 = ((kq + 1) & 3) * 128, s2 = ((kq + 2) & 3) * 128, s3 = ((kq + 3) & 3) * 128;
        const f32x4 r0 = (__builtin_shufflevector(own, own, 0, 1, 2, 3) + rr[s1]) + (rr[s2] + rr[s3]), r1 = (__builtin_shufflevector(own, own, 4, 5, 6, 7) + rr[s1 + 64]) + (rr[s2 + 64] + rr[s3 + 64]);
        *(u32x4*)(out + (size_t)(64 * i3 + 32 * tt + (lane & 31)) * 1024 + g3 * 128 + kq * 32 + (lane >> 5) * 16) =
            join32(cvtpk(r0[0], r0[1]), cvtpk(r0[2], r0[3]), cvtpk(r1[0], r1[1]), cvtpk(r1[2], r1[3]));
    }
}
}

extern "C" void kernel_launch(void* const* d_in, const int* in_sizes, int n_in, void* d_out, int out_size, void* d_ws, size_t ws_size, hipStream_t stream) {
    fused_kernel<<<256, 512, 0, stream>>>((const char*)d_in[0], (const char*)d_in[1], (const char*)d_in[2], (const char*)d_in[4], (const char*)d_in[5],
                                          (const char*)d_in[6], (char*)d_out, (char*)d_ws);
}
```

```cpp
#include <hip/hip_runtime.h>
#include <hip/hip_bf16.h>
#include <stdint.h>

#define GAS __attribute__((address_space(1)))
#define LAS __attribute__((address_space(3)))
#define RLX_AGENT __ATOMIC_RELAXED, __HIP_MEMORY_SCOPE_AGENT
typedef unsigned u32x2 __attribute__((ext_vector_type(2)));
typedef unsigned u32x4 __attribute__((ext_vector_type(4)));
typedef float f32x2 __attribute__((ext_vector_type(2)));
typedef float f32x4 __attribute__((ext_vector_type(4)));
typedef float f32x8 __attribute__((ext_vector_type(8)));
typedef float f32x16 __attribute__((ext_vector_type(16)));
typedef __bf16 bf16x2 __attribute__((ext_vector_type(2)));
typedef __bf16 bf16x8 __attribute__((ext_vector_type(8)));
typedef __bf16 bf16x16 __attribute__((ext_vector_type(16)));
typedef short s16x4 __attribute__((ext_vector_type(4)));
typedef GAS unsigned long long gu64;
typedef LAS const char* lds_cptr;

namespace {
constexpr unsigned WS_EPOCH = 0;
constexpr int FS = 8;
constexpr unsigned WS_KVFLAG = 11u << 20;
constexpr unsigned WS_OAFLAG = WS_KVFLAG + 1024 * FS * 8;
constexpr unsigned WS_OBFLAG = WS_OAFLAG + 1024 * FS * 8;
constexpr unsigned WS_GATE = 294912;
constexpr unsigned WS_Q = 1u << 20;
constexpr unsigned WS_KV = WS_Q + (2u << 20);
constexpr unsigned WS_OA = WS_KV + (4u << 20);
constexpr unsigned WS_OB = WS_OA + (2u << 20);
constexpr unsigned WS_END = WS_OB + (2u << 20);
static_assert(WS_END <= WS_KVFLAG, "ws");
constexpr int X_OFF = 0, XKS = 80 * 128, WR_OFF = 8 * XKS, WSLOT = 24576;
constexpr int GW_OFF = WR_OFF + 3 * WSLOT, EP_OFF = GW_OFF + 1024, PROG_OFF = EP_OFF + 256, DROP_OFF = EP_OFF + 512;
constexpr int ONES_OFF = 159744, LDS_BYTES = 163840;
constexpr int SPARE_OFF = 131072;
static_assert(DROP_OFF + 256 <= ONES_OFF && SPARE_OFF + 8 * 3072 <= GW_OFF, "lds");
constexpr float QSCALE = 0.25f * 1.4426950408889634f;
constexpr unsigned kSpinCap = 1u << 18;
constexpr int GKS = 2, GVS = 4;
constexpr int PFKB = 21;

#define SBAR() __builtin_amdgcn_sched_barrier(0)
#define PIN(x) asm volatile("" : "+v"(x))

__device__ __forceinline__ unsigned cvtpk(float lo, float hi) { return __builtin_bit_cast(unsigned, __builtin_convertvector((f32x2){lo, hi}, bf16x2)); }
#define GLDS_OPEN "s_mov_b32 %0, m0\n\ts_mov_b32 m0, %3\n\ts_nop 2\n\t"
#define GLDS_ARGS : "=&s"(sv) : "v"(voff), "s"(sbase), "s"(ldsb) : "memory"
__device__ __forceinline__ void glds1(const void* sbase, unsigned voff, unsigned ldsb) { unsigned sv; asm volatile(GLDS_OPEN "global_load_lds_dwordx4 %1, %2\n\ts_mov_b32 m0, %0" GLDS_ARGS); }
__device__ __forceinline__ void glds3(const void* sbase, unsigned voff, unsigned v1, unsigned v2, unsigned ldsb) { unsigned sv;
    asm volatile(GLDS_OPEN "global_load_lds_dwordx4 %1, %2\n\tglobal_load_lds_dwordx4 %4, %2 offset:1024\n\tglobal_load_lds_dwordx4 %5, %2 offset:2048\n\ts_mov_b32 m0, %0"
                 : "=&s"(sv) : "v"(voff), "s"(sbase), "s"(ldsb), "v"(v1), "v"(v2) : "memory"); }
__device__ __forceinline__ void glds4c(const void* sbase, unsigned voff, unsigned ldsb) { unsigned sv;
    asm volatile(GLDS_OPEN "global_load_lds_dwordx4 %1, %2 sc1\n\tglobal_load_lds_dwordx4 %1, %2 offset:1024 sc1\n\tglobal_load_lds_dwordx4 %1, %2 offset:2048 sc1\n\tglobal_load_lds_dwordx4 %1, %2 offset:3072 sc1\n\ts_mov_b32 m0, %0" GLDS_ARGS); }
__device__ __forceinline__ void touch(const void* sbase, unsigned voff, unsigned ldsb) { unsigned sv; asm volatile(GLDS_OPEN "global_load_lds_dword %1, %2\n\ts_mov_b32 m0, %0" GLDS_ARGS); }
__device__ __forceinline__ void glds2c(const void* sbase, unsigned voff, unsigned ldsb) { unsigned sv;
    asm volatile(GLDS_OPEN "global_load_lds_dwordx4 %1, %2 sc1\n\tglobal_load_lds_dwordx4 %1, %2 offset:1024 sc1\n\ts_mov_b32 m0, %0" GLDS_ARGS); }
__device__ __forceinline__ u32x2 tr64(lds_cptr p) { return __builtin_bit_cast(u32x2, __builtin_amdgcn_ds_read_tr16_b64_v4i16((LAS s16x4*)p)); }
__device__ __forceinline__ u32x4 tr128(lds_cptr p) { const u32x2 a = tr64(p), b = tr64(p + 512); return (u32x4){a[0], a[1], b[0], b[1]}; }
__device__ __forceinline__ u32x4 lds128(lds_cptr p) { return *(LAS const u32x4*)p; }
__device__ __forceinline__ f32x4 mfma16(u32x4 a, u32x4 b, f32x4 c) { return __builtin_amdgcn_mfma_f32_16x16x32_bf16(__builtin_bit_cast(bf16x8, a), __builtin_bit_cast(bf16x8, b), c, 0, 0, 0); }
__device__ __forceinline__ f32x4 mfma16k16(u32x2 a, u32x2 b, f32x4 c) { return __builtin_amdgcn_mfma_f32_16x16x16bf16_1k(__builtin_bit_cast(s16x4, a), __builtin_bit_cast(s16x4, b), c, 0, 0, 0); }
__device__ __forceinline__ f32x16 mfma32(u32x4 a, u32x4 b, f32x16 c) { return __builtin_amdgcn_mfma_f32_32x32x16_bf16(__builtin_bit_cast(bf16x8, a), __builtin_bit_cast(bf16x8, b), c, 0, 0, 0); }
__device__ __forceinline__ f32x16 smf(u32x4 a, f32x8 b, f32x16 c, int idx) { return __builtin_amdgcn_smfmac_f32_32x32x32_bf16(__builtin_bit_cast(bf16x8, a), __builtin_bit_cast(bf16x16, b), c, idx, 0, 0); }
__device__ __forceinline__ u32x2 pack4(f32x4 v) { return (u32x2){cvtpk(v[0], v[1]), cvtpk(v[2], v[3])}; }
__device__ __forceinline__ u32x4 join16(u32x2 x, u32x2 y) {
    auto a = __builtin_amdgcn_permlane16_swap(x[0], y[0], false, false); auto b = __builtin_amdgcn_permlane16_swap(x[1], y[1], false, false);
    return (u32x4){a[0], b[0], a[1], b[1]}; }
__device__ __forceinline__ u32x4 join32(unsigned a0, unsigned a1, unsigned b0, unsigned b1) {
    auto p0 = __builtin_amdgcn_permlane32_swap(a0, b0, false, false); auto p1 = __builtin_amdgcn_permlane32_swap(a1, b1, false, false);
    return (u32x4){p0[0], p1[0], p0[1], p1[1]}; }
__device__ __forceinline__ float mx(float a, float b) { return __builtin_elementwise_maximum(a, b); }
__device__ __forceinline__ float xmax16(float v) { auto r = __builtin_amdgcn_permlane16_swap(__float_as_uint(v), __float_as_uint(v), false, false); return mx(__uint_as_float(r[0]), __uint_as_float(r[1])); }
__device__ __forceinline__ float xmax32(float v) { auto r = __builtin_amdgcn_permlane32_swap(__float_as_uint(v), __float_as_uint(v), false, false); return mx(__uint_as_float(r[0]), __uint_as_float(r[1])); }
__device__ __forceinline__ f32x8 lo8(const f32x16& v) { return __builtin_shufflevector(v, v, 0, 1, 2, 3, 4, 5, 6, 7); }
__device__ __forceinline__ f32x8 hi8(const f32x16& v) { return __builtin_shufflevector(v, v, 8, 9, 10, 11, 12, 13, 14, 15); }

template <int N> struct IC { static constexpr int v = N; };
template <int B, int E, class F> __device__ __forceinline__ void static_for(F&& f) { if constexpr (B < E) { f(IC<B>{}); static_for<B + 1, E>(f); } }
template <int N> __device__ __forceinline__ void wait_bar() { asm volatile("s_waitcnt vmcnt(%0) lgkmcnt(0)\n\ts_barrier" :: "i"(N) : "memory"); }
struct Frags { u32x4 x[2][5], w[2][3]; };

__global__ __launch_bounds__(512, 2) void fused_kernel(const char* __restrict__ x, const char* __restrict__ wqa, const char* __restrict__ woa,
                                                       const char* __restrict__ wqb, const char* __restrict__ wob, const char* __restrict__ gw,
                                                       char* __restrict__ out, char* ws) {
    __shared__ __attribute__((aligned(256))) char lds[LDS_BYTES];
    const int tid = threadIdx.x, lane = tid & 63, w = __builtin_amdgcn_readfirstlane(tid >> 6);
    const int xcd = blockIdx.x & 7, idx = blockIdx.x >> 3;
    const int g = 2 * (xcd >> 1) + (idx & 1), i = 16 * (xcd & 1) + (idx >> 1);
    const int h = 4 * ((xcd + 4) & 7) + (idx & 3), j = idx >> 2;
    const int i3 = 4 * xcd + (idx & 3), g3 = idx >> 2;
    const unsigned lds0 = (unsigned)(uintptr_t)(LAS char*)lds;
    const lds_cptr L = (lds_cptr)lds;
    const __amdgpu_buffer_rsrc_t wsr = __builtin_amdgcn_make_buffer_rsrc(ws, 0, WS_END, 0x00020000);
    const int lane16 = lane * 16, l15 = lane & 15, lg = lane >> 4, wq = w & 3;
#define LDC(soff) __builtin_amdgcn_raw_buffer_load_b128(wsr, lane16, (soff), 16)
#define STC(v, soff) __builtin_amdgcn_raw_buffer_store_b128((v), wsr, lane16 + (soff), 0, 16)
#define BAR() asm volatile("s_waitcnt lgkmcnt(0)\n\ts_barrier" ::: "memory")
#define VM0() asm volatile("s_waitcnt vmcnt(0)" ::: "memory")
    const int rot = (i + 4 * (g & 1) + (g >> 1)) & 7;
#define KS(s) (((s) + rot) & 7)
    auto mktag = [&](unsigned long long ev) { const unsigned long long ep = ((unsigned long long)__builtin_amdgcn_readfirstlane((unsigned)(ev >> 32)) << 32) | (unsigned)__builtin_amdgcn_readfirstlane((unsigned)ev);
        unsigned long long z = (ep + 0x9E3779B97F4A7C15ull) * 0xBF58476D1CE4E5B9ull; z ^= z >> 29; return ((z ^ ep) >> 32) ? z : ~z; };
    unsigned long long tag;
    const int xsw = (l15 >> 1) & 7;
    const lds_cptr xrd0 = L + X_OFF + l15 * 128 + ((lg ^ xsw) * 16), xrd1 = L + X_OFF + l15 * 128 + (((lg + 4) ^ xsw) * 16);
    const unsigned xv0 = (lane >> 3) * 1024 + (((lane & 7) ^ lg) * 16), xv1 = (xv0 ^ 64) + 8192;
    const int gk = (g + GKS) & 7, gv_ = (g + GVS) & 7;
    const unsigned wvl = (lane >> 3) * 3072 + (((lane & 7) ^ ((lg & 1) << 1)) * 16);
    auto first = [&](int s) { const unsigned v_ = wvl ^ ((w & 1) * 64); glds3(wqb + (size_t)((KS(s) * 64 + 8 * w) * 3072), v_ + g * 128, v_ + gk * 128, v_ + gv_ * 128, lds0 + WR_OFF + s * WSLOT + w * 3072);
                              glds1(x + (size_t)(64 * i + 8 * w) * 1024 + KS(s) * 128, xv0 ^ ((w & 1) * 64), lds0 + X_OFF + s * XKS + (16 + 8 * w) * 128); };
    first(0);
    { const char* pc = (const char*)__builtin_amdgcn_s_getpc();
      touch(pc - ((uintptr_t)pc & 127), ((idx * 8 + w) * 4 + (lane & 3)) % (PFKB * 8) * 128, lds0 + DROP_OFF); }
    first(1);
    touch(wqa + blockIdx.x * 6144, (w * 6 + (lane < 6 ? lane : 5)) * 128, lds0 + DROP_OFF);
#define SETW(n, v) do { if (lane == 0) __hip_atomic_store((LAS int*)(lds + PROG_OFF) + (n), (v), __ATOMIC_RELAXED, __HIP_MEMORY_SCOPE_WORKGROUP); } while (0)
#define WAITW(n, v) do { while (!__all(__hip_atomic_load((LAS int*)(lds + PROG_OFF) + (n) + (lane & 3), __ATOMIC_RELAXED, __HIP_MEMORY_SCOPE_WORKGROUP) >= (v))) __builtin_amdgcn_s_sleep(1); asm volatile("" ::: "memory"); } while (0)
    const char* kvsrc = ws + WS_KV + h * 131072 + wq * 8192;
#define KVLD(b) do { glds4c(kvsrc + (b) * 32768, lane16, lds0 + (b) * 32768 + wq * 8192); glds4c(kvsrc + (b) * 32768 + 4096, lane16, lds0 + (b) * 32768 + wq * 8192 + 4096); } while (0)
    if (w >= 4) {
        __builtin_amdgcn_s_setprio(3);
        const unsigned wv0 = wvl + 16 * wq * 3072, wv1 = (wv0 ^ 64) + 8 * 3072;
        const unsigned wdst = lds0 + WR_OFF + wq * 6144;
        auto issue = [&](auto S) { constexpr int s = S.v;
            if constexpr (s == 8 || s == 9) {
                const int st = 4 * (s - 8) + wq; const char* hs = x + (size_t)(i ? 64 * i - 16 : 0) * 1024 + KS(st) * 128;
                glds1(hs, xv0, lds0 + X_OFF + st * XKS); glds1(hs, xv1, lds0 + X_OFF + st * XKS + 1024); }
            const char* wsrc = (s < 8 ? wqb : wqa) + (size_t)(KS(s) * 64 * 3072);
            const int c1 = s < 8 ? gk : g, c2 = s < 8 ? gv_ : g;
            glds3(wsrc, wv0 + g * 128, wv0 + c1 * 128, wv0 + c2 * 128, wdst + (s % 3) * WSLOT); glds3(wsrc, wv1 + g * 128, wv1 + c1 * 128, wv1 + c2 * 128, wdst + (s % 3) * WSLOT + 3072); };
        wait_bar<5 + TCH>();
        if (w == 4) glds1(gw, lane16, lds0 + GW_OFF);
        if (w == 5) { const void* sbase = ws + WS_EPOCH; const unsigned voff = (lane & 1) * 4, ldsb = lds0 + EP_OFF; unsigned sv; asm volatile(GLDS_OPEN "global_load_lds_dword %1, %2 sc1\n\ts_mov_b32 m0, %0" GLDS_ARGS); }
        issue(IC<2>{});
        f32x4 ga = {};
        const lds_cptr gwrd = L + GW_OFF + lg * 16;
        static_for<0, 14>([&](auto S) { constexpr int s = S.v;
            constexpr int cnt[14] = {6, 6, 6, 6, 6, 6, 8, 63, 6, 6, 6, 6, 6, 6};
            wait_bar<cnt[s]>();
            u32x4 g0, g1, g2, g3;
            if constexpr (s >= 4 && s < 12) if (g == (i & 7)) { constexpr int c = s - 4;
                g0 = lds128(gwrd + KS(c) * 128); g1 = lds128(xrd0 + c * XKS + (1 + wq) * 2048); g2 = lds128(gwrd + KS(c) * 128 + 64); g3 = lds128(xrd1 + c * XKS + (1 + wq) * 2048); }
            if constexpr (s + 3 < 16) issue(IC<s + 3>{});
            if constexpr (s == 7) tag = mktag(*(LAS const unsigned long long*)(lds + EP_OFF));
            if constexpr (s >= 4 && s < 12) if (g == (i & 7)) { ga = mfma16(g0, g1, ga); ga = mfma16(g2, g3, ga); }
            if constexpr (s == 7) wait_bar<14>();
        });
        VM0();
        SETW(4 + wq, 1);
        if (g == (i & 7) && lane < 16) __hip_atomic_store((gu64*)(ws + WS_GATE) + 64 * i + 16 * wq + lane, (tag & 0xffffffff00000000ull) | __float_as_uint(1.f / (1.f + __expf(-ga[0]))), RLX_AGENT);
        { gu64* f0 = (gu64*)(ws + WS_KVFLAG) + (((h - 4 * (lane < 32 ? GKS : GVS)) & 31) * 32 + (lane & 31)) * FS; gu64* f1 = (gu64*)(ws + WS_KVFLAG) + (h * 32 + 4 * j + wq) * FS;
#pragma unroll 1
          for (unsigned spins = 0; spins < kSpinCap; ++spins) {
              const unsigned long long a = __hip_atomic_load(f0, RLX_AGENT), b = __hip_atomic_load(f1, RLX_AGENT);
              if (__all(a == tag && b == tag)) break;
              __builtin_amdgcn_s_sleep(1); }
          __builtin_amdgcn_fence(__ATOMIC_ACQUIRE, "wavefront"); }
        KVLD(0);
        WAITW(0, 1);
        glds2c(ws + WS_Q + (h * 64 + 8 * j + 2 * wq) * 1024, lane16, lds0 + SPARE_OFF + wq * 2048); KVLD(1);
        wait_bar<0>();
    } else {
        const char* xsrc = x + (size_t)(64 * i + 16 * w) * 1024; const unsigned xdst = lds0 + X_OFF + (16 + 16 * w) * 128;
#define XLD(s) do { glds1(xsrc + KS(s) * 128, xv0, xdst + (s) * XKS); glds1(xsrc + KS(s) * 128, xv1, xdst + (s) * XKS + 1024); } while (0)
        { const unsigned ov = (l15 == 0 || l15 == 4) ? 0x3F803F80u : 0u; *(LAS u32x4*)(lds + ONES_OFF + tid * 16) = (u32x4){ov, ov, ov, ov}; }
        if (tid < 24) *(LAS int*)(lds + PROG_OFF + tid * 4) = 0;
        const lds_cptr wrd = L + WR_OFF + lg * 3072 + ((lane >> 2) & 3) * 128 + ((w ^ (((lane >> 3) & 1) | ((lg & 1) << 1))) * 32) + (lane & 3) * 8;
        auto readf = [&](auto S, Frags& f) { constexpr int s = S.v;
#pragma unroll
            for (int cc = 0; cc < 2; ++cc) {
#pragma unroll
                for (int sec = 0; sec < 3; ++sec) f.w[cc][sec] = tr128(wrd + (s % 3) * WSLOT + cc * 12288 + sec * 1024);
#pragma unroll
                for (int t = (s < 8 ? 1 : 0); t < 5; ++t) f.x[cc][t] = lds128((cc ? xrd1 : xrd0) + (s & 7) * XKS + t * 2048);
            } };
        f32x4 mc, mp, mp0;
#pragma unroll
        for (int r = 0; r < 4; ++r) { const int dc = l15 - (4 * lg + r); mc[r] = (dc >= 0 && !(dc & 1)) ? 0.f : -3e38f; mp[r] = (dc <= -2 && !(dc & 1)) ? 0.f : -3e38f; mp0[r] = i ? mp[r] : -3e38f; }
        PIN(mc); PIN(mp); PIN(mp0);
        f32x4 q[5] = {}, k[5] = {}, v[5] = {};
        Frags fr[2];
        wait_bar<5 + TCH>();
        XLD(2); XLD(3);
        readf(IC<0>{}, fr[0]);
        auto mm = [&](auto S, const Frags& f) { constexpr int s = S.v;
#pragma unroll
            for (int cc = 0; cc < 2; ++cc)
#pragma unroll
                for (int t = (s < 8 ? 1 : 0); t < 5; ++t) { k[t] = mfma16(f.w[cc][1], f.x[cc][t], k[t]); v[t] = mfma16(f.x[cc][t], f.w[cc][2], v[t]); if (t) q[t] = mfma16(f.w[cc][0], f.x[cc][t], q[t]); } };
        static_for<0, 16>([&](auto S) { constexpr int s = S.v;
            constexpr int cnt[14] = {5, 4, 4, 4, 4, 2, 0, 63, 63, 63, 63, 63, 63, 63};
            if constexpr (s == 7) {
                BAR(); SBAR();
                mm(S, fr[1]);
                const unsigned dst = WS_KV + w * 131072 + (i >> 1) * 8192 + (i & 1) * 2048;
#pragma unroll
                for (int t = 0; t < 2; ++t) {
                    STC(join16(pack4(k[1 + 2 * t]), pack4(k[2 + 2 * t])), dst + gk * 524288 + t * 1024);
                    STC(join16(pack4(v[1 + 2 * t]), pack4(v[2 + 2 * t])), dst + gv_ * 524288 + 4096 + t * 1024);
                    STC(join16(pack4(q[1 + 2 * t] * QSCALE), pack4(q[2 + 2 * t] * QSCALE)), WS_Q + ((4 * g + w) * 64 + 2 * i + t) * 1024); }
#pragma unroll
                for (int t = 0; t < 5; ++t) q[t] = k[t] = v[t] = (f32x4){};
                SBAR();
            }
            if constexpr (s < 14) { wait_bar<cnt[s]>(); }
            else if constexpr (s == 14) { asm volatile("s_waitcnt lgkmcnt(0)" ::: "memory"); SETW(w, 1); WAITW(4, 1); }
            SBAR();
            if constexpr (s < 15) readf(IC<s + 1>{}, fr[(s + 1) & 1]);
            if constexpr (s != 7) mm(S, fr[s & 1]);
            if constexpr (s < 15 && s != 7) {
#pragma unroll
                for (int u = 0; u < 22; ++u) { __builtin_amdgcn_sched_group_barrier(0x8, 1, 0); __builtin_amdgcn_sched_group_barrier(0x100, 1, 0); }
                __builtin_amdgcn_sched_group_barrier(0x8, 8, 0); }
            SBAR();
            if constexpr (s < 4) XLD(s + 4);
            if constexpr (s == 4) tag = mktag(*(LAS const unsigned long long*)(lds + EP_OFF));
            if constexpr (s == 9) { VM0(); if (lane == 0) __hip_atomic_store((gu64*)(ws + WS_KVFLAG) + ((4 * g + w) * 32 + i) * FS, tag, RLX_AGENT); }
        });
        u32x2 kf[5], vf[5], of[4];
#pragma unroll
        for (int t = 0; t < 5; ++t) { kf[t] = pack4(k[t]); vf[t] = pack4(v[t]); }
        const u32x2 one2 = {0x3F803F80u, 0x3F803F80u};
#pragma unroll
        for (int sb = 0; sb < 4; ++sb) {
            const f32x4 z = {}; const u32x2 qs = pack4(q[sb + 1] * QSCALE);
            f32x4 sp = mfma16k16(kf[sb], qs, sb ? mp : mp0), sc = mfma16k16(kf[sb + 1], qs, mc);
            float m = mx(mx(mx(mx(sc[0], sc[1]), sc[2]), mx(mx(sc[3], sp[0]), sp[1])), mx(sp[2], sp[3]));
            m = xmax32(xmax16(m));
#pragma unroll
            for (int r = 0; r < 4; ++r) { sc[r] = __builtin_amdgcn_exp2f(sc[r] - m); sp[r] = __builtin_amdgcn_exp2f(sp[r] - m); }
            const u32x2 pp = pack4(sp), pc = pack4(sc);
            f32x4 l = mfma16k16(one2, pp, z); l = mfma16k16(one2, pc, l);
            f32x4 o = mfma16k16(vf[sb], pp, z); o = mfma16k16(vf[sb + 1], pc, o);
            o *= __builtin_amdgcn_rcpf(l[0]);
            of[sb] = pack4(o);
        }
        STC(join16(of[0], of[1]), WS_OA + (2 * i * 32 + 4 * g + w) * 1024);
        STC(join16(of[2], of[3]), WS_OA + ((2 * i + 1) * 32 + 4 * g + w) * 1024);
        BAR();
        __builtin_amdgcn_s_setprio(3);
        KVLD(2); KVLD(3);
    }
    const u32x4 qa = lds128(L + SPARE_OFF + wq * 2048 + lane16), qb = lds128(L + SPARE_OFF + wq * 2048 + 1024 + lane16);
    const int tt = w & 1, kq = w >> 1, kh = w >> 2;
    const unsigned osrc = ((2 * i3 + tt) * 32 + 8 * kq) * 1024;
    const unsigned ovoff = (lane >> 3) * 1024 + g3 * 128 + (((lane & 7) ^ (((lane >> 4) & 1) << 2)) * 16);
    u32x4 of[8];
    unsigned long long gg, fb;
    gu64* obf = (gu64*)(ws + WS_OBFLAG) + ((2 * i3 + tt) * 32 + 8 * kq) * FS;
#define POLLB() asm volatile("s_nop 4\n\tglobal_load_dwordx2 %0, %1, %2 sc1" : "=v"(fb) : "v"((lane & 7) * 8 * FS), "s"(obf) : "memory")
    float gv;
    const lds_cptr wo0 = L + kq * 16384 + (8 * (lane >> 5) + ((lane >> 2) & 3)) * 128 + (((lane >> 3) & 1) * 64) + (lg & 1) * 32 + (lane & 3) * 8;
    const lds_cptr wo1 = L + kq * 16384 + (8 * (lane >> 5) + ((lane >> 2) & 3)) * 128 + ((1 ^ ((lane >> 3) & 1)) * 64) + (lg & 1) * 32 + (lane & 3) * 8;
    f32x16 a0 = {}, a1 = {};
    auto proj_a = [&]() {
#pragma unroll
            for (int u = 0; u < 8; ++u) { a0 = mfma32(tr128(wo0 + u * 2048), of[u], a0); a1 = mfma32(tr128(wo1 + u * 2048), of[u], a1); }
            a0 *= gv; a1 *= gv; PIN(a0); PIN(a1); };
    const int sidx = (int)0xDDDDDDDDu;
    const bool real = (lane & 16) == 0;
    auto finish = [&](f32x16 o, const u32x4 qf, int T) {
        float chk = o[8];
#pragma unroll
        for (int r = 0; r < 8; ++r) chk = mx(chk, fabsf(o[r]) * 0x1p-15f);
        if (__builtin_expect(__any(!(chk < 0x1p90f)), 0)) {
            const f32x16 zero = {}; float m = -3e38f;
            const unsigned ksrc = WS_KV + h * 131072;
#pragma unroll 1
            for (int t = 0; t < 64; ++t) { f32x16 s = mfma32(LDC(ksrc + (t >> 2) * 8192 + (t & 3) * 1024), qf, zero);
#pragma unroll
                for (int r = 0; r < 16; ++r) m = fmaxf(m, s[r]); }
            m = xmax32(m); o = zero;
            const unsigned vl = (lane >> 5) * 512 + l15 * 16;
            const u32x4 ones = {0x3F803F80u, 0x3F803F80u, 0x3F803F80u, 0x3F803F80u};
#pragma unroll 1
            for (int t = 0; t < 64; ++t) { const unsigned ts = ksrc + (t >> 2) * 8192 + (t & 3) * 1024; f32x16 s = mfma32(LDC(ts), qf, zero);
                const u32x4 a = __builtin_amdgcn_raw_buffer_load_b128(wsr, vl, ts + 4096, 16), c = __builtin_amdgcn_raw_buffer_load_b128(wsr, vl + 256, ts + 4096, 16);
#pragma unroll
                for (int r = 0; r < 16; ++r) s[r] = __builtin_amdgcn_exp2f(s[r] - m);
                o = smf(real ? a : ones, lo8(s), o, sidx); o = smf(real ? c : ones, hi8(s), o, sidx); }
        }
        const float rl = __builtin_amdgcn_rcpf(o[8]);
        STC(join32(cvtpk(o[0] * rl, o[1] * rl), cvtpk(o[2] * rl, o[3] * rl), cvtpk(o[4] * rl, o[5] * rl), cvtpk(o[6] * rl, o[7] * rl)), WS_OB + (T * 32 + h) * 1024);
    };
    {
        unsigned kba = lds0 + kh * 8192 + lane16, vba = lds0 + (real ? kh * 8192 + 4096 : ONES_OFF) + (lane >> 5) * 512 + l15 * 16;
        const int vstep = real ? 16384 : 0;
        f32x16 sa, sb, ca, cb, oa, ob;
        u32x4 kf = lds128(L + lane16), va = lds128((lds_cptr)(uintptr_t)vba), vc = va;
        { const f32x16 zero = {}; u32x4 z4 = {}, e0 = {lane < 32 ? 0x3F80u : 0u, 0u, 0u, 0u}; PIN(z4);
          oa = mfma32(z4, z4, zero); ob = mfma32(z4, e0, zero);
          sa = mfma32(kf, qa, zero); sb = mfma32(kf, qb, zero); kf = lds128((lds_cptr)(uintptr_t)kba);
          float ma = sa[0], mb = sb[0];
#pragma unroll
          for (int r = 1; r < 16; ++r) { ma = mx(ma, sa[r]); mb = mx(mb, sb[r]); }
          sb = mfma32(e0, z4, zero);
          ca = mfma32(e0, (u32x4){__float_as_uint(-40.f - xmax32(ma)) >> 16, 0u, 0u, 0u}, zero); cb = mfma32(e0, (u32x4){__float_as_uint(-40.f - xmax32(mb)) >> 16, 0u, 0u, 0u}, zero); }
        if (w >= 4) SETW(w, 2);
        sa = mfma32(kf, qa, ca);
        auto run = [&](int n) {
            asm volatile("s_waitcnt lgkmcnt(0)\n\ts_nop 11\n"
            "1:\n\t"
            "v_exp_f32 v32, v32\n\tv_exp_f32 v33, v33\n\tv_smfmac_f32_32x32x32_bf16 %[ob], %[vc], v[56:63], %[ix]\n\tds_read_b128 %[vc], %[vb] offset:256\n\tv_exp_f32 v34, v34\n\tv_exp_f32 v35, v35\n\tv_exp_f32 v36, v36\n\tv_exp_f32 v37, v37\n\tv_mfma_f32_32x32x16_bf16 v[48:63], %[kf], %[qb], %[cb]\n\tds_read_b128 %[kf], %[kb] offset:1024\n\tv_exp_f32 v38, v38\n\tv_exp_f32 v39, v39\n\tv_exp_f32 v40, v40\n\tv_exp_f32 v41, v41\n\ts_waitcnt lgkmcnt(2)\n\tv_smfmac_f32_32x32x32_bf16 %[oa], %[va], v[32:39], %[ix]\n\tv_exp_f32 v42, v42\n\tv_exp_f32 v43, v43\n\tv_exp_f32 v44, v44\n\tv_exp_f32 v45, v45\n\tv_exp_f32 v46, v46\n\tv_exp_f32 v47, v47\n\t"
            "v_exp_f32 v48, v48\n\tv_exp_f32 v49, v49\n\ts_waitcnt lgkmcnt(0)\n\tv_smfmac_f32_32x32x32_bf16 %[oa], %[vc], v[40:47], %[ix]\n\tv_exp_f32 v50, v50\n\tv_exp_f32 v51, v51\n\tv_exp_f32 v52, v52\n\tv_exp_f32 v53, v53\n\tv_mfma_f32_32x32x16_bf16 v[32:47], %[kf], %[qa], %[ca]\n\tv_exp_f32 v54, v54\n\tv_exp_f32 v55, v55\n\tv_exp_f32 v56, v56\n\tv_exp_f32 v57, v57\n\tv_smfmac_f32_32x32x32_bf16 %[ob], %[va], v[48:55], %[ix]\n\tds_read_b128 %[va], %[vb] offset:1024\n\tv_exp_f32 v58, v58\n\tv_exp_f32 v59, v59\n\tv_exp_f32 v60, v60\n\tv_exp_f32 v61, v61\n\tv_exp_f32 v62, v62\n\tv_exp_f32 v63, v63\n\t"
            "v_exp_f32 v32, v32\n\tv_exp_f32 v33, v33\n\tv_smfmac_f32_32x32x32_bf16 %[ob], %[vc], v[56:63], %[ix]\n\tds_read_b128 %[vc], %[vb] offset:1280\n\tv_exp_f32 v34, v34\n\tv_exp_f32 v35, v35\n\tv_exp_f32 v36, v36\n\tv_exp_f32 v37, v37\n\tv_mfma_f32_32x32x16_bf16 v[48:63], %[kf], %[qb], %[cb]\n\tds_read_b128 %[kf], %[kb] offset:2048\n\tv_exp_f32 v38, v38\n\tv_exp_f32 v39, v39\n\tv_exp_f32 v40, v40\n\tv_exp_f32 v41, v41\n\ts_waitcnt lgkmcnt(2)\n\tv_smfmac_f32_32x32x32_bf16 %[oa], %[va], v[32:39], %[ix]\n\tv_exp_f32 v42, v42\n\tv_exp_f32 v43, v43\n\tv_exp_f32 v44, v44\n\tv_exp_f32 v45, v45\n\tv_exp_f32 v46, v46\n\tv_exp_f32 v47, v47\n\t"
            "v_exp_f32 v48, v48\n\tv_exp_f32 v49, v49\n\ts_waitcnt lgkmcnt(0)\n\tv_smfmac_f32_32x32x32_bf16 %[oa], %[vc], v[40:47], %[ix]\n\tv_exp_f32 v50, v50\n\tv_exp_f32 v51, v51\n\tv_exp_f32 v52, v52\n\tv_exp_f32 v53, v53\n\tv_mfma_f32_32x32x16_bf16 v[32:47], %[kf], %[qa], %[ca]\n\tv_exp_f32 v54, v54\n\tv_exp_f32 v55, v55\n\tv_exp_f32 v56, v56\n\tv_exp_f32 v57, v57\n\tv_smfmac_f32_32x32x32_bf16 %[ob], %[va], v[48:55], %[ix]\n\tds_read_b128 %[va], %[vb] offset:2048\n\tv_exp_f32 v58, v58\n\tv_exp_f32 v59, v59\n\tv_exp_f32 v60, v60\n\tv_exp_f32 v61, v61\n\tv_exp_f32 v62, v62\n\tv_exp_f32 v63, v63\n\t"
            "v_exp_f32 v32, v32\n\tv_exp_f32 v33, v33\n\tv_smfmac_f32_32x32x32_bf16 %[ob], %[vc], v[56:63], %[ix]\n\tds_read_b128 %[vc], %[vb] offset:2304\n\tv_exp_f32 v34, v34\n\tv_exp_f32 v35, v35\n\tv_exp_f32 v36, v36\n\tv_exp_f32 v37, v37\n\tv_mfma_f32_32x32x16_bf16 v[48:63], %[kf], %[qb], %[cb]\n\tds_read_b128 %[kf], %[kb] offset:3072\n\tv_exp_f32 v38, v38\n\tv_exp_f32 v39, v39\n\tv_exp_f32 v40, v40\n\tv_exp_f32 v41, v41\n\ts_waitcnt lgkmcnt(2)\n\tv_smfmac_f32_32x32x32_bf16 %[oa], %[va], v[32:39], %[ix]\n\tv_exp_f32 v42, v42\n\tv_exp_f32 v43, v43\n\tv_exp_f32 v44, v44\n\tv_exp_f32 v45, v45\n\tv_exp_f32 v46, v46\n\tv_exp_f32 v47, v47\n\t"
            "v_exp_f32 v48, v48\n\tv_exp_f32 v49, v49\n\ts_waitcnt lgkmcnt(0)\n\tv_smfmac_f32_32x32x32_bf16 %[oa], %[vc], v[40:47], %[ix]\n\tv_exp_f32 v50, v50\n\tv_exp_f32 v51, v51\n\tv_exp_f32 v52, v52\n\tv_exp_f32 v53, v53\n\tv_mfma_f32_32x32x16_bf16 v[32:47], %[kf], %[qa], %[ca]\n\tv_exp_f32 v54, v54\n\tv_exp_f32 v55, v55\n\tv_exp_f32 v56, v56\n\tv_exp_f32 v57, v57\n\tv_smfmac_f32_32x32x32_bf16 %[ob], %[va], v[48:55], %[ix]\n\tds_read_b128 %[va], %[vb] offset:3072\n\tv_exp_f32 v58, v58\n\tv_exp_f32 v59, v59\n\tv_exp_f32 v60, v60\n\tv_exp_f32 v61, v61\n\tv_exp_f32 v62, v62\n\tv_exp_f32 v63, v63\n\t"
            "v_exp_f32 v32, v32\n\tv_exp_f32 v33, v33\n\tv_smfmac_f32_32x32x32_bf16 %[ob], %[vc], v[56:63], %[ix]\n\tds_read_b128 %[vc], %[vb] offset:3328\n\tv_exp_f32 v34, v34\n\tv_exp_f32 v35, v35\n\tv_exp_f32 v36, v36\n\tv_exp_f32 v37, v37\n\tv_mfma_f32_32x32x16_bf16 v[48:63], %[kf], %[qb], %[cb]\n\tds_read_b128 %[kf], %[kb] offset:16384\n\tv_exp_f32 v38, v38\n\tv_exp_f32 v39, v39\n\tv_exp_f32 v40, v40\n\tv_exp_f32 v41, v41\n\ts_waitcnt lgkmcnt(2)\n\tv_smfmac_f32_32x32x32_bf16 %[oa], %[va], v[32:39], %[ix]\n\tv_exp_f32 v42, v42\n\tv_exp_f32 v43, v43\n\tv_exp_f32 v44, v44\n\tv_exp_f32 v45, v45\n\tv_exp_f32 v46, v46\n\tv_exp_f32 v47, v47\n\t"
            "v_exp_f32 v48, v48\n\tv_exp_f32 v49, v49\n\ts_waitcnt lgkmcnt(0)\n\tv_smfmac_f32_32x32x32_bf16 %[oa], %[vc], v[40:47], %[ix]\n\tv_exp_f32 v50, v50\n\tv_exp_f32 v51, v51\n\tv_exp_f32 v52, v52\n\tv_exp_f32 v53, v53\n\tv_mfma_f32_32x32x16_bf16 v[32:47], %[kf], %[qa], %[ca]\n\tv_exp_f32 v54, v54\n\tv_exp_f32 v55, v55\n\tv_exp_f32 v56, v56\n\tv_exp_f32 v57, v57\n\tv_add_u32 %[vb], %[vb], %[vs]\n\tv_smfmac_f32_32x32x32_bf16 %[ob], %[va], v[48:55], %[ix]\n\tds_read_b128 %[va], %[vb] offset:0\n\tv_add_u32 %[kb], 0x4000, %[kb]\n\ts_sub_u32 %[n], %[n], 1\n\tv_exp_f32 v58, v58\n\tv_exp_f32 v59, v59\n\tv_exp_f32 v60, v60\n\tv_exp_f32 v61, v61\n\tv_exp_f32 v62, v62\n\tv_exp_f32 v63, v63\n\t"
            "s_cmp_lg_u32 %[n], 0\n\ts_cbranch_scc1 1b\n\ts_waitcnt lgkmcnt(0)\n\ts_nop 7"
            : "+{v[32:47]}"(sa), "+{v[48:63]}"(sb), [oa] "+v"(oa), [ob] "+v"(ob), [kf] "+v"(kf), [va] "+v"(va), [vc] "+v"(vc), [kb] "+v"(kba), [vb] "+v"(vba), [n] "+s"(n)
            : [qa] "v"(qa), [qb] "v"(qb), [ca] "v"(ca), [cb] "v"(cb), [vs] "v"(vstep), [ix] "v"(sidx) : "scc", "memory");
        };
        run(2);
        __builtin_amdgcn_s_setprio(2);
        run(2);
        if (w < 4) { VM0(); if (lane == 0) __hip_atomic_store((gu64*)(ws + WS_OAFLAG) + (i * 32 + 4 * g + w) * FS, tag, RLX_AGENT); }
        SETW(8 + w, 1);
        WAITW(8, 1);
        kf = lds128((lds_cptr)(uintptr_t)kba); va = lds128((lds_cptr)(uintptr_t)vba); sa = mfma32(kf, qa, ca);
        if (w >= 4) WAITW(12, 1); else WAITW(4, 2);
#pragma unroll
        for (int u = 0; u < 8; ++u) glds1(woa + (size_t)(64 * (2 * wq + kh) + 8 * u) * 1024, ovoff, lds0 + (2 * wq + kh) * 8192 + u * 1024);
        if (w < 2) touch(wob + g3 * 128, (128 * (idx & 3) + 64 * w + lane) * 1024, lds0 + DROP_OFF);
        __builtin_amdgcn_s_setprio(1);
        run(1);
#pragma unroll 1
        for (unsigned spins = 0; spins < kSpinCap; ++spins) {
            const unsigned long long f = __hip_atomic_load((gu64*)(ws + WS_OAFLAG) + (i3 * 32 + 8 * kq + (lane & 7)) * FS, RLX_AGENT);
            gg = __hip_atomic_load((gu64*)(ws + WS_GATE) + 64 * i3 + 32 * tt + (lane & 31), RLX_AGENT);
            if (__all(f == tag && (gg >> 32) == (tag >> 32))) break;
            __builtin_amdgcn_s_sleep(1); }
        VM0();
#pragma unroll
        for (int u = 0; u < 8; ++u) of[u] = LDC(WS_OA + osrc + u * 1024);
        SETW(8 + w, 2);
        __builtin_amdgcn_s_setprio(0);
        run(3);
#pragma unroll
        for (int u = 0; u < 8; ++u) PIN(of[u]);
        if (w >= 4) POLLB();
        ob = smf(vc, hi8(sb), ob, sidx);
        f32x16 give, o;
#pragma unroll
        for (int r = 0; r < 16; ++r) { give[r] = r < 9 ? (kh ? oa[r] : ob[r]) : 0.f; o[r] = r < 9 ? (kh ? ob[r] : oa[r]) : 0.f; }
        LAS char* xw = (LAS char*)lds + SPARE_OFF + w * 3072 + lane16;
        *(LAS f32x4*)xw = __builtin_shufflevector(give, give, 0, 1, 2, 3); *(LAS f32x4*)(xw + 1024) = __builtin_shufflevector(give, give, 4, 5, 6, 7); *(LAS float*)(xw + 2048) = give[8];
        gv = __uint_as_float((unsigned)gg);
        if (w < 4) {
            WAITW(8, 2); WAITW(12, 2);
            proj_a();
            SETW(8 + w, 3); WAITW(8, 3);
#pragma unroll
            for (int u = 0; u < 16; ++u) glds1(wob + (size_t)(128 * w + 8 * u) * 1024, ovoff, lds0 + (w < 2 ? w * 16384 + u * 1024 : w * 32768 + (u >> 3) * 16384 + (u & 7) * 1024));
            VM0(); POLLB(); }
        BAR();
        LAS const char* xr = L + SPARE_OFF + (w ^ 4) * 3072 + lane16;
        const f32x4 r0 = *(LAS const f32x4*)xr, r1 = *(LAS const f32x4*)(xr + 1024);
#pragma unroll
        for (int r = 0; r < 4; ++r) { o[r] += r0[r]; o[4 + r] += r1[r]; }
        o[8] += *(LAS const float*)(xr + 2048);
        finish(o, kh ? qb : qa, 8 * j + 2 * wq + kh);
    }
    {
        f32x16 b0 = {}, b1 = {};
        u32x4 wf[16], ob8[8];
        asm volatile("s_waitcnt vmcnt(0)" : "+v"(fb) :: "memory");
        if (lane == 0) __hip_atomic_store((gu64*)(ws + WS_OBFLAG) + ((8 * j + 2 * wq + kh) * 32 + h) * FS, tag, RLX_AGENT);
        unsigned got = (unsigned)__builtin_amdgcn_ballot_w64(fb == tag) & 0xffu;
        const char* pb = ws + WS_OB + osrc + 4096;
        asm volatile("s_nop 4\n\t"
            "s_bitcmp1_b32 %[got], 0\n\ts_cbranch_scc0 10f\n\tglobal_load_dwordx4 %0, %[vo], %[pb] offset:-4096 sc1\n10:\n\t"
            "s_bitcmp1_b32 %[got], 1\n\ts_cbranch_scc0 11f\n\tglobal_load_dwordx4 %1, %[vo], %[pb] offset:-3072 sc1\n11:\n\t"
            "s_bitcmp1_b32 %[got], 2\n\ts_cbranch_scc0 12f\n\tglobal_load_dwordx4 %2, %[vo], %[pb] offset:-2048 sc1\n12:\n\t"
            "s_bitcmp1_b32 %[got], 3\n\ts_cbranch_scc0 13f\n\tglobal_load_dwordx4 %3, %[vo], %[pb] offset:-1024 sc1\n13:\n\t"
            "s_bitcmp1_b32 %[got], 4\n\ts_cbranch_scc0 14f\n\tglobal_load_dwordx4 %4, %[vo], %[pb] offset:0 sc1\n14:\n\t"
            "s_bitcmp1_b32 %[got], 5\n\ts_cbranch_scc0 15f\n\tglobal_load_dwordx4 %5, %[vo], %[pb] offset:1024 sc1\n15:\n\t"
            "s_bitcmp1_b32 %[got], 6\n\ts_cbranch_scc0 16f\n\tglobal_load_dwordx4 %6, %[vo], %[pb] offset:2048 sc1\n16:\n\t"
            "s_bitcmp1_b32 %[got], 7\n\ts_cbranch_scc0 17f\n\tglobal_load_dwordx4 %7, %[vo], %[pb] offset:3072 sc1\n17:\n\t"
            : "=&v"(ob8[0]), "=&v"(ob8[1]), "=&v"(ob8[2]), "=&v"(ob8[3]), "=&v"(ob8[4]), "=&v"(ob8[5]), "=&v"(ob8[6]), "=&v"(ob8[7])
            : [got] "s"(got), [pb] "s"(pb), [vo] "v"(lane16) : "scc", "memory");
        if (w >= 4) { proj_a(); SETW(8 + w, 3); }
        const int wb0 = kq < 2 ? 0 : kq * 16384, wb1 = kq < 2 ? 0 : kq * 16384 + 8192;
#pragma unroll
        for (int u = 0; u < 8; ++u) { wf[2 * u] = tr128(wo0 + (u < 4 ? wb0 : wb1) + u * 2048); wf[2 * u + 1] = tr128(wo1 + (u < 4 ? wb0 : wb1) + u * 2048); }
        { unsigned fresh, spins = kSpinCap;
          asm volatile("s_nop 4\n\t"
            "s_cmp_eq_u32 %[got], 0xff\n\ts_cbranch_scc1 3f\n2:\n\t"
            "global_load_dwordx2 %[fb], %[fo], %[fp] sc1\n\ts_waitcnt vmcnt(0)\n\t"
            "v_cmp_eq_u64 vcc, %[tg], %[fb]\n\ts_nop 1\n\ts_and_b32 %[fr], vcc_lo, 0xff\n\ts_andn2_b32 %[fr], %[fr], %[got]\n\t"
            "s_bitcmp1_b32 %[fr], 0\n\ts_cbranch_scc0 10f\n\tglobal_load_dwordx4 %0, %[vo], %[pb] offset:-4096 sc1\n10:\n\t"
            "s_bitcmp1_b32 %[fr], 1\n\ts_cbranch_scc0 11f\n\tglobal_load_dwordx4 %1, %[vo], %[pb] offset:-3072 sc1\n11:\n\t"
            "s_bitcmp1_b32 %[fr], 2\n\ts_cbranch_scc0 12f\n\tglobal_load_dwordx4 %2, %[vo], %[pb] offset:-2048 sc1\n12:\n\t"
            "s_bitcmp1_b32 %[fr], 3\n\ts_cbranch_scc0 13f\n\tglobal_load_dwordx4 %3, %[vo], %[pb] offset:-1024 sc1\n13:\n\t"
            "s_bitcmp1_b32 %[fr], 4\n\ts_cbranch_scc0 14f\n\tglobal_load_dwordx4 %4, %[vo], %[pb] offset:0 sc1\n14:\n\t"
            "s_bitcmp1_b32 %[fr], 5\n\ts_cbranch_scc0 15f\n\tglobal_load_dwordx4 %5, %[vo], %[pb] offset:1024 sc1\n15:\n\t"
            "s_bitcmp1_b32 %[fr], 6\n\ts_cbranch_scc0 16f\n\tglobal_load_dwordx4 %6, %[vo], %[pb] offset:2048 sc1\n16:\n\t"
            "s_bitcmp1_b32 %[fr], 7\n\ts_cbranch_scc0 17f\n\tglobal_load_dwordx4 %7, %[vo], %[pb] offset:3072 sc1\n17:\n\t"
            "s_or_b32 %[got], %[got], %[fr]\n\ts_cmp_eq_u32 %[got], 0xff\n\ts_cbranch_scc1 3f\n\ts_sleep 1\n\ts_sub_u32 %[sp], %[sp], 1\n\ts_cbranch_scc0 2b\n"
            "3:\n\ts_waitcnt vmcnt(0)"
            : "+v"(ob8[0]), "+v"(ob8[1]), "+v"(ob8[2]), "+v"(ob8[3]), "+v"(ob8[4]), "+v"(ob8[5]), "+v"(ob8[6]), "+v"(ob8[7]), [got] "+s"(got), [fr] "=&s"(fresh), [sp] "+s"(spins), [fb] "=&v"(fb)
            : [pb] "s"(pb), [vo] "v"(lane16), [fo] "v"((lane & 7) * 8 * FS), [fp] "s"(obf), [tg] "s"(tag) : "scc", "vcc", "memory"); }
#pragma unroll
        for (int u = 0; u < 8; ++u) { b0 = mfma32(wf[2 * u], ob8[u], b0); b1 = mfma32(wf[2 * u + 1], ob8[u], b1); }
        a0 += b0 * (1.f - gv); a1 += b1 * (1.f - gv);
        WAITW(12, 3);
        const int cs = tt ? 1024 : 512;
        LAS f32x4* rw = (LAS f32x4*)(lds + (tt ? 73728 : 32768) + kq * 2048 + lane16);
        if (kq != 0) { rw[0] = __builtin_shufflevector(a0, a0, 0, 1, 2, 3); rw[64] = __builtin_shufflevector(a0, a0, 4, 5, 6, 7); }
        if (kq != 1) { rw[cs] = __builtin_shufflevector(a0, a0, 8, 9, 10, 11); rw[cs + 64] = __builtin_shufflevector(a0, a0, 12, 13, 14, 15); }
        if (kq != 2) { rw[2 * cs] = __builtin_shufflevector(a1, a1, 0, 1, 2, 3); rw[2 * cs + 64] = __builtin_shufflevector(a1, a1, 4, 5, 6, 7); }
        if (kq != 3) { rw[3 * cs] = __builtin_shufflevector(a1, a1, 8, 9, 10, 11); rw[3 * cs + 64] = __builtin_shufflevector(a1, a1, 12, 13, 14, 15); }
        const f32x16 am = kq < 2 ? a0 : a1; f32x8 own = (kq & 1) ? hi8(am) : lo8(am);
        unsigned oo = (64 * i3 + 32 * tt + (lane & 31)) * 1024 + g3 * 128 + kq * 32 + (lane >> 5) * 16, ra = lds0 + (tt ? 73728 : 32768) + kq * cs * 16 + lane16;
        unsigned ra1 = ra + ((kq + 1) & 3) * 2048, ra2 = ra + ((kq + 2) & 3) * 2048, ra3 = ra + ((kq + 3) & 3) * 2048;
        PIN(own); PIN(oo); PIN(ra1); PIN(ra2); PIN(ra3);
        BAR();
        LAS const f32x4 *p1 = (LAS const f32x4*)(uintptr_t)ra1, *p2 = (LAS const f32x4*)(uintptr_t)ra2, *p3 = (LAS const f32x4*)(uintptr_t)ra3;
        const f32x4 r0 = (__builtin_shufflevector(own, own, 0, 1, 2, 3) + p1[0]) + (p2[0] + p3[0]), r1 = (__builtin_shufflevector(own, own, 4, 5, 6, 7) + p1[64]) + (p2[64] + p3[64]);
        *(u32x4*)(out + oo) =
            join32(cvtpk(r0[0], r0[1]), cvtpk(r0[2], r0[3]), cvtpk(r1[0], r1[1]), cvtpk(r1[2], r1[3]));
        if (blockIdx.x == 0 && tid == 0) __hip_atomic_store((gu64*)(ws + WS_EPOCH), tag, RLX_AGENT);
    }
}
}

extern "C" void kernel_launch(void* const* d_in, const int* in_sizes, int n_in, void* d_out, int out_size, void* d_ws, size_t ws_size, hipStream_t stream) {
    fused_kernel<<<256, 512, 0, stream>>>((const char*)d_in[0], (const char*)d_in[1], (const char*)d_in[2], (const char*)d_in[4], (const char*)d_in[5],
                                          (const char*)d_in[6], (char*)d_out, (char*)d_ws);
}
```

```cpp
#include <hip/hip_runtime.h>
#include <hip/hip_bf16.h>
#include <stdint.h>

#define GAS __attribute__((address_space(1)))
#define LAS __attribute__((address_space(3)))
#define RLX_AGENT __ATOMIC_RELAXED, __HIP_MEMORY_SCOPE_AGENT
typedef unsigned u32x2 __attribute__((ext_vector_type(2)));
typedef unsigned u32x4 __attribute__((ext_vector_type(4)));
typedef float f32x2 __attribute__((ext_vector_type(2)));
typedef float f32x4 __attribute__((ext_vector_type(4)));
typedef float f32x8 __attribute__((ext_vector_type(8)));
typedef float f32x16 __attribute__((ext_vector_type(16)));
typedef __bf16 bf16x2 __attribute__((ext_vector_type(2)));
typedef __bf16 bf16x8 __attribute__((ext_vector_type(8)));
typedef __bf16 bf16x16 __attribute__((ext_vector_type(16)));
typedef short s16x4 __attribute__((ext_vector_type(4)));
typedef GAS unsigned long long gu64;
typedef LAS const char* lds_cptr;

namespace {
constexpr unsigned WS_EPOCH = 0;
constexpr int FS = 8;
constexpr unsigned WS_KVFLAG = 11u << 20;
constexpr unsigned WS_OAFLAG = WS_KVFLAG + 1024 * FS * 8;
constexpr unsigned WS_OBFLAG = WS_OAFLAG + 1024 * FS * 8;
constexpr unsigned WS_GATE = 294912;
constexpr unsigned WS_Q = 1u << 20;
constexpr unsigned WS_KV = WS_Q + (2u << 20);
constexpr unsigned WS_OA = WS_KV + (4u << 20);
constexpr unsigned WS_OB = WS_OA + (2u << 20);
constexpr unsigned WS_END = WS_OB + (2u << 20);
static_assert(WS_END <= WS_KVFLAG, "ws");
constexpr int X_OFF = 0, XKS = 80 * 128, WR_OFF = 8 * XKS, WSLOT = 24576;
constexpr int GW_OFF = WR_OFF + 3 * WSLOT, EP_OFF = GW_OFF + 1024, PROG_OFF = EP_OFF + 256, DROP_OFF = EP_OFF + 512;
constexpr int ONES_OFF = 159744, LDS_BYTES = 163840;
constexpr int SPARE_OFF = 131072;
static_assert(DROP_OFF + 256 <= ONES_OFF && SPARE_OFF + 8 * 3072 <= GW_OFF, "lds");
constexpr float QSCALE = 0.25f * 1.4426950408889634f;
constexpr unsigned kSpinCap = 1u << 18;
constexpr int GKS = 2, GVS = 4;
constexpr int PFKB = 24;

#define SBAR() __builtin_amdgcn_sched_barrier(0)
#define PIN(x) asm volatile("" : "+v"(x))

__device__ __forceinline__ unsigned cvtpk(float lo, float hi) { return __builtin_bit_cast(unsigned, __builtin_convertvector((f32x2){lo, hi}, bf16x2)); }
#define GLDS_OPEN "s_mov_b32 %0, m0\n\ts_mov_b32 m0, %3\n\ts_nop 2\n\t"
#define GLDS_ARGS : "=&s"(sv) : "v"(voff), "s"(sbase), "s"(ldsb) : "memory"
__device__ __forceinline__ void glds1(const void* sbase, unsigned voff, unsigned ldsb) { unsigned sv; asm volatile(GLDS_OPEN "global_load_lds_dwordx4 %1, %2\n\ts_mov_b32 m0, %0" GLDS_ARGS); }
__device__ __forceinline__ void glds3(const void* sbase, unsigned voff, unsigned v1, unsigned v2, unsigned ldsb) { unsigned sv;
    asm volatile(GLDS_OPEN "global_load_lds_dwordx4 %1, %2\n\tglobal_load_lds_dwordx4 %4, %2 offset:1024\n\tglobal_load_lds_dwordx4 %5, %2 offset:2048\n\ts_mov_b32 m0, %0"
                 : "=&s"(sv) : "v"(voff), "s"(sbase), "s"(ldsb), "v"(v1), "v"(v2) : "memory"); }
__device__ __forceinline__ void glds4c(const void* sbase, unsigned voff, unsigned ldsb) { unsigned sv;
    asm volatile(GLDS_OPEN "global_load_lds_dwordx4 %1, %2 sc1\n\tglobal_load_lds_dwordx4 %1, %2 offset:1024 sc1\n\tglobal_load_lds_dwordx4 %1, %2 offset:2048 sc1\n\tglobal_load_lds_dwordx4 %1, %2 offset:3072 sc1\n\ts_mov_b32 m0, %0" GLDS_ARGS); }
__device__ __forceinline__ void touch(const void* sbase, unsigned voff, unsigned ldsb) { unsigned sv; asm volatile(GLDS_OPEN "global_load_lds_dword %1, %2\n\ts_mov_b32 m0, %0" GLDS_ARGS); }
__device__ __forceinline__ void glds2c(const void* sbase, unsigned voff, unsigned ldsb) { unsigned sv;
    asm volatile(GLDS_OPEN "global_load_lds_dwordx4 %1, %2 sc1\n\tglobal_load_lds_dwordx4 %1, %2 offset:1024 sc1\n\ts_mov_b32 m0, %0" GLDS_ARGS); }
__device__ __forceinline__ u32x2 tr64(lds_cptr p) { return __builtin_bit_cast(u32x2, __builtin_amdgcn_ds_read_tr16_b64_v4i16((LAS s16x4*)p)); }
__device__ __forceinline__ u32x4 tr128(lds_cptr p) { const u32x2 a = tr64(p), b = tr64(p + 512); return (u32x4){a[0], a[1], b[0], b[1]}; }
__device__ __forceinline__ u32x4 lds128(lds_cptr p) { return *(LAS const u32x4*)p; }
__device__ __forceinline__ f32x4 mfma16(u32x4 a, u32x4 b, f32x4 c) { return __builtin_amdgcn_mfma_f32_16x16x32_bf16(__builtin_bit_cast(bf16x8, a), __builtin_bit_cast(bf16x8, b), c, 0, 0, 0); }
__device__ __forceinline__ f32x4 mfma16k16(u32x2 a, u32x2 b, f32x4 c) { return __builtin_amdgcn_mfma_f32_16x16x16bf16_1k(__builtin_bit_cast(s16x4, a), __builtin_bit_cast(s16x4, b), c, 0, 0, 0); }
__device__ __forceinline__ f32x16 mfma32(u32x4 a, u32x4 b, f32x16 c) { return __builtin_amdgcn_mfma_f32_32x32x16_bf16(__builtin_bit_cast(bf16x8, a), __builtin_bit_cast(bf16x8, b), c, 0, 0, 0); }
__device__ __forceinline__ f32x16 smf(u32x4 a, f32x8 b, f32x16 c, int idx) { return __builtin_amdgcn_smfmac_f32_32x32x32_bf16(__builtin_bit_cast(bf16x8, a), __builtin_bit_cast(bf16x16, b), c, idx, 0, 0); }
__device__ __forceinline__ u32x2 pack4(f32x4 v) { return (u32x2){cvtpk(v[0], v[1]), cvtpk(v[2], v[3])}; }
__device__ __forceinline__ u32x4 join16(u32x2 x, u32x2 y) {
    auto a = __builtin_amdgcn_permlane16_swap(x[0], y[0], false, false); auto b = __builtin_amdgcn_permlane16_swap(x[1], y[1], false, false);
    return (u32x4){a[0], b[0], a[1], b[1]}; }
__device__ __forceinline__ u32x4 join32(unsigned a0, unsigned a1, unsigned b0, unsigned b1) {
    auto p0 = __builtin_amdgcn_permlane32_swap(a0, b0, false, false); auto p1 = __builtin_amdgcn_permlane32_swap(a1, b1, false, false);
    return (u32x4){p0[0], p1[0], p0[1], p1[1]}; }
__device__ __forceinline__ float mx(float a, float b) { return __builtin_elementwise_maximum(a, b); }
__device__ __forceinline__ float xmax16(float v) { auto r = __builtin_amdgcn_permlane16_swap(__float_as_uint(v), __float_as_uint(v), false, false); return mx(__uint_as_float(r[0]), __uint_as_float(r[1])); }
__device__ __forceinline__ float xmax32(float v) { auto r = __builtin_amdgcn_permlane32_swap(__float_as_uint(v), __float_as_uint(v), false, false); return mx(__uint_as_float(r[0]), __uint_as_float(r[1])); }
__device__ __forceinline__ f32x8 lo8(const f32x16& v) { return __builtin_shufflevector(v, v, 0, 1, 2, 3, 4, 5, 6, 7); }
__device__ __forceinline__ f32x8 hi8(const f32x16& v) { return __builtin_shufflevector(v, v, 8, 9, 10, 11, 12, 13, 14, 15); }

template <int N> struct IC { static constexpr int v = N; };
template <int B, int E, class F> __device__ __forceinline__ void static_for(F&& f) { if constexpr (B < E) { f(IC<B>{}); static_for<B + 1, E>(f); } }
template <int N> __device__ __forceinline__ void wait_bar() { asm volatile("s_waitcnt vmcnt(%0) lgkmcnt(0)\n\ts_barrier" :: "i"(N) : "memory"); }
struct Frags { u32x4 x[2][5], w[2][3]; };

__global__ __launch_bounds__(512, 2) void fused_kernel(const char* __restrict__ x, const char* __restrict__ wqa, const char* __restrict__ woa,
                                                       const char* __restrict__ wqb, const char* __restrict__ wob, const char* __restrict__ gw,
                                                       char* __restrict__ out, char* ws) {
    __shared__ __attribute__((aligned(256))) char lds[LDS_BYTES];
    const int tid = threadIdx.x, lane = tid & 63, w = __builtin_amdgcn_readfirstlane(tid >> 6);
    const int xcd = blockIdx.x & 7, idx = blockIdx.x >> 3;
    const int g = 2 * (xcd >> 1) + (idx & 1), i = 16 * (xcd & 1) + (idx >> 1);
    const int h = 4 * ((xcd + 4) & 7) + (idx & 3), j = idx >> 2;
    const int i3 = 4 * xcd + (idx & 3), g3 = idx >> 2;
    const unsigned lds0 = (unsigned)(uintptr_t)(LAS char*)lds;
    const lds_cptr L = (lds_cptr)lds;
    const __amdgpu_buffer_rsrc_t wsr = __builtin_amdgcn_make_buffer_rsrc(ws, 0, WS_END, 0x00020000);
    const int lane16 = lane * 16, l15 = lane & 15, lg = lane >> 4, wq = w & 3;
#define LDC(soff) __builtin_amdgcn_raw_buffer_load_b128(wsr, lane16, (soff), 16)
#define STC(v, soff) __builtin_amdgcn_raw_buffer_store_b128((v), wsr, lane16 + (soff), 0, 16)
#define BAR() asm volatile("s_waitcnt lgkmcnt(0)\n\ts_barrier" ::: "memory")
#define VM0() asm volatile("s_waitcnt vmcnt(0)" ::: "memory")
    const int rot = (i + 4 * (g & 1) + (g >> 1)) & 7;
#define KS(s) (((s) + rot) & 7)
    auto mktag = [&](unsigned long long ev) { const unsigned long long ep = ((unsigned long long)__builtin_amdgcn_readfirstlane((unsigned)(ev >> 32)) << 32) | (unsigned)__builtin_amdgcn_readfirstlane((unsigned)ev);
        unsigned long long z = (ep + 0x9E3779B97F4A7C15ull) * 0xBF58476D1CE4E5B9ull; z ^= z >> 29; return ((z ^ ep) >> 32) ? z : ~z; };
    unsigned long long tag;
    const int xsw = (l15 >> 1) & 7;
    const lds_cptr xrd0 = L + X_OFF + l15 * 128 + ((lg ^ xsw) * 16), xrd1 = L + X_OFF + l15 * 128 + (((lg + 4) ^ xsw) * 16);
    const unsigned xv0 = (lane >> 3) * 1024 + (((lane & 7) ^ lg) * 16), xv1 = (xv0 ^ 64) + 8192;
    const int gk = (g + GKS) & 7, gv_ = (g + GVS) & 7;
    const unsigned wvl = (lane >> 3) * 3072 + (((lane & 7) ^ ((lg & 1) << 1)) * 16);
    auto first = [&](int s) { const unsigned v_ = wvl ^ ((w & 1) * 64); glds3(wqb + (size_t)((KS(s) * 64 + 8 * w) * 3072), v_ + g * 128, v_ + gk * 128, v_ + gv_ * 128, lds0 + WR_OFF + s * WSLOT + w * 3072);
                              glds1(x + (size_t)(64 * i + 8 * w) * 1024 + KS(s) * 128, xv0 ^ ((w & 1) * 64), lds0 + X_OFF + s * XKS + (16 + 8 * w) * 128); };
    first(0);
    { const char* pc = (const char*)__builtin_amdgcn_s_getpc();
      touch(pc - ((uintptr_t)pc & 127), ((idx * 8 + w) * 4 + (lane & 3)) % (PFKB * 8) * 128, lds0 + DROP_OFF); }
    first(1);
    touch(wqa + blockIdx.x * 6144, (w * 6 + (lane < 6 ? lane : 5)) * 128, lds0 + DROP_OFF);
#define SETW(n, v) do { asm volatile("" ::: "memory"); if (lane == 0) __hip_atomic_store((LAS int*)(lds + PROG_OFF) + (n), (v), __ATOMIC_RELAXED, __HIP_MEMORY_SCOPE_WORKGROUP); } while (0)
#define WAITM(n, m, v) do { _Pragma("unroll 1") for (unsigned sp_ = 0; sp_ < (kSpinCap << 6) && !__all(__hip_atomic_load((LAS int*)(lds + PROG_OFF) + (n) + (lane & (m)), __ATOMIC_RELAXED, __HIP_MEMORY_SCOPE_WORKGROUP) >= (v)); ++sp_) __builtin_amdgcn_s_sleep(1); asm volatile("" ::: "memory"); } while (0)
#define WAITW(n, v) WAITM(n, 3, v)
    const char* kvsrc = ws + WS_KV + h * 131072 + wq * 8192;
#define KVLD(b) do { glds4c(kvsrc + (b) * 32768, lane16, lds0 + (b) * 32768 + wq * 8192); glds4c(kvsrc + (b) * 32768 + 4096, lane16, lds0 + (b) * 32768 + wq * 8192 + 4096); } while (0)
    if (w >= 4) {
        __builtin_amdgcn_s_setprio(3);
        const unsigned wv0 = wvl + 16 * wq * 3072, wv1 = (wv0 ^ 64) + 8 * 3072;
        const unsigned wdst = lds0 + WR_OFF + wq * 6144;
        auto issue = [&](auto S) { constexpr int s = S.v;
            if constexpr (s == 8 || s == 9) {
                const int st = 4 * (s - 8) + wq; const char* hs = x + (size_t)(i ? 64 * i - 16 : 0) * 1024 + KS(st) * 128;
                glds1(hs, xv0, lds0 + X_OFF + st * XKS); glds1(hs, xv1, lds0 + X_OFF + st * XKS + 1024); }
            const char* wsrc = (s < 8 ? wqb : wqa) + (size_t)(KS(s) * 64 * 3072);
            const int c1 = s < 8 ? gk : g, c2 = s < 8 ? gv_ : g;
            glds3(wsrc, wv0 + g * 128, wv0 + c1 * 128, wv0 + c2 * 128, wdst + (s % 3) * WSLOT); glds3(wsrc, wv1 + g * 128, wv1 + c1 * 128, wv1 + c2 * 128, wdst + (s % 3) * WSLOT + 3072); };
        wait_bar<6>();
        if (w == 4) glds1(gw, lane16, lds0 + GW_OFF);
        if (w == 5) { const void* sbase = ws + WS_EPOCH; const unsigned voff = (lane & 1) * 4, ldsb = lds0 + EP_OFF; unsigned sv; asm volatile(GLDS_OPEN "global_load_lds_dword %1, %2 sc1\n\ts_mov_b32 m0, %0" GLDS_ARGS); }
        issue(IC<2>{});
        f32x4 ga = {};
        const lds_cptr gwrd = L + GW_OFF + lg * 16;
        static_for<0, 14>([&](auto S) { constexpr int s = S.v;
            constexpr int cnt[14] = {7, 6, 6, 6, 6, 6, 8, 63, 6, 6, 6, 6, 6, 6};
            wait_bar<cnt[s]>();
            u32x4 g0, g1, g2, g3;
            if constexpr (s >= 4 && s < 12) if (g == (i & 7)) { constexpr int c = s - 4;
                g0 = lds128(gwrd + KS(c) * 128); g1 = lds128(xrd0 + c * XKS + (1 + wq) * 2048); g2 = lds128(gwrd + KS(c) * 128 + 64); g3 = lds128(xrd1 + c * XKS + (1 + wq) * 2048); }
            if constexpr (s + 3 < 16) issue(IC<s + 3>{});
            if constexpr (s == 7) tag = mktag(*(LAS const unsigned long long*)(lds + EP_OFF));
            if constexpr (s >= 4 && s < 12) if (g == (i & 7)) { ga = mfma16(g0, g1, ga); ga = mfma16(g2, g3, ga); }
            if constexpr (s == 7) wait_bar<14>();
        });
        VM0();
        SETW(4 + wq, 1);
        if (g == (i & 7) && lane < 16) __hip_atomic_store((gu64*)(ws + WS_GATE) + 64 * i + 16 * wq + lane, (tag & 0xffffffff00000000ull) | __float_as_uint(1.f / (1.f + __expf(-ga[0]))), RLX_AGENT);
        { gu64* f0 = (gu64*)(ws + WS_KVFLAG) + (((h - 4 * (lane < 32 ? GKS : GVS)) & 31) * 32 + (lane & 31)) * FS; gu64* f1 = (gu64*)(ws + WS_KVFLAG) + (h * 32 + 4 * j + wq) * FS;
#pragma unroll 1
          for (unsigned spins = 0; spins < kSpinCap; ++spins) {
              const unsigned long long a = __hip_atomic_load(f0, RLX_AGENT), b = __hip_atomic_load(f1, RLX_AGENT);
              if (__all(a == tag && b == tag)) break;
              __builtin_amdgcn_s_sleep(1); }
          __builtin_amdgcn_fence(__ATOMIC_ACQUIRE, "wavefront"); }
        KVLD(0);
        WAITW(0, 1);
        glds2c(ws + WS_Q + (h * 64 + 8 * j + 2 * wq) * 1024, lane16, lds0 + SPARE_OFF + wq * 2048); KVLD(1);
        asm volatile("s_waitcnt vmcnt(8)" ::: "memory");
        { const f32x16 zero = {}; const u32x4 kf = lds128(L + wq * 8192 + lane16);
          const f32x16 s0 = mfma32(kf, lds128(L + SPARE_OFF + wq * 2048 + lane16), zero), s1 = mfma32(kf, lds128(L + SPARE_OFF + wq * 2048 + 1024 + lane16), zero);
          float m0 = s0[0], m1 = s1[0];
#pragma unroll
          for (int r = 1; r < 16; ++r) { m0 = mx(m0, s0[r]); m1 = mx(m1, s1[r]); }
          *(LAS u32x2*)(lds + SPARE_OFF + 8192 + wq * 512 + lane * 8) = (u32x2){__float_as_uint(-40.f - xmax32(m0)) >> 16, __float_as_uint(-40.f - xmax32(m1)) >> 16}; }
        wait_bar<0>();
    } else {
        const char* xsrc = x + (size_t)(64 * i + 16 * w) * 1024; const unsigned xdst = lds0 + X_OFF + (16 + 16 * w) * 128;
#define XLD(s) do { glds1(xsrc + KS(s) * 128, xv0, xdst + (s) * XKS); glds1(xsrc + KS(s) * 128, xv1, xdst + (s) * XKS + 1024); } while (0)
        { const unsigned ov = (l15 == 0 || l15 == 4) ? 0x3F803F80u : 0u; *(LAS u32x4*)(lds + ONES_OFF + tid * 16) = (u32x4){ov, ov, ov, ov}; }
        if (tid < 24) *(LAS int*)(lds + PROG_OFF + tid * 4) = 0;
        const lds_cptr wrd = L + WR_OFF + lg * 3072 + ((lane >> 2) & 3) * 128 + ((w ^ (((lane >> 3) & 1) | ((lg & 1) << 1))) * 32) + (lane & 3) * 8;
        auto readf = [&](auto S, Frags& f) { constexpr int s = S.v;
#pragma unroll
            for (int cc = 0; cc < 2; ++cc) {
#pragma unroll
                for (int sec = 0; sec < 3; ++sec) f.w[cc][sec] = tr128(wrd + (s % 3) * WSLOT + cc * 12288 + sec * 1024);
#pragma unroll
                for (int t = (s < 8 ? 1 : 0); t < 5; ++t) f.x[cc][t] = lds128((cc ? xrd1 : xrd0) + (s & 7) * XKS + t * 2048);
            } };
        f32x4 mc, mp, mp0;
#pragma unroll
        for (int r = 0; r < 4; ++r) { const int dc = l15 - (4 * lg + r); mc[r] = (dc >= 0 && !(dc & 1)) ? 0.f : -3e38f; mp[r] = (dc <= -2 && !(dc & 1)) ? 0.f : -3e38f; mp0[r] = i ? mp[r] : -3e38f; }
        u32x2 bc = {(l15 >= 4 * lg ? 0xffffu : 0u) | (l15 >= 4 * lg + 1 ? 0xffff0000u : 0u), (l15 >= 4 * lg + 2 ? 0xffffu : 0u) | (l15 >= 4 * lg + 3 ? 0xffff0000u : 0u)};
        PIN(bc); PIN(mc); PIN(mp); PIN(mp0);
        f32x4 q[5] = {}, k[5] = {}, v[5] = {};
        Frags fr[2];
        wait_bar<6>();
        XLD(2); XLD(3);
        readf(IC<0>{}, fr[0]);
        auto mm = [&](auto S, const Frags& f) { constexpr int s = S.v;
#pragma unroll
            for (int cc = 0; cc < 2; ++cc)
#pragma unroll
                for (int t = (s < 8 ? 1 : 0); t < 5; ++t) { k[t] = mfma16(f.w[cc][1], f.x[cc][t], k[t]); v[t] = mfma16(f.x[cc][t], f.w[cc][2], v[t]); if (t) q[t] = mfma16(f.w[cc][0], f.x[cc][t], q[t]); } };
        static_for<0, 16>([&](auto S) { constexpr int s = S.v;
            constexpr int cnt[14] = {5, 4, 4, 4, 4, 2, 0, 63, 63, 63, 63, 63, 63, 63};
            if constexpr (s == 7) {
                BAR(); SBAR();
                mm(S, fr[1]);
                const unsigned dst = WS_KV + w * 131072 + (i >> 1) * 8192 + (i & 1) * 2048;
#pragma unroll
                for (int t = 0; t < 2; ++t) {
                    STC(join16(pack4(k[1 + 2 * t]), pack4(k[2 + 2 * t])), dst + gk * 524288 + t * 1024);
                    STC(join16(pack4(v[1 + 2 * t]), pack4(v[2 + 2 * t])), dst + gv_ * 524288 + 4096 + t * 1024);
                    STC(join16(pack4(q[1 + 2 * t] * QSCALE), pack4(q[2 + 2 * t] * QSCALE)), WS_Q + ((4 * g + w) * 64 + 2 * i + t) * 1024); }
#pragma unroll
                for (int t = 0; t < 5; ++t) q[t] = k[t] = v[t] = (f32x4){};
                SBAR();
            }
            if constexpr (s < 14) { wait_bar<cnt[s]>(); }
            else if constexpr (s == 14) { asm volatile("s_waitcnt lgkmcnt(0)" ::: "memory"); SETW(w, 1); WAITW(4, 1); }
            SBAR();
            if constexpr (s < 15) readf(IC<s + 1>{}, fr[(s + 1) & 1]);
            if constexpr (s != 7) mm(S, fr[s & 1]);
            if constexpr (s < 15 && s != 7) {
#pragma unroll
                for (int u = 0; u < 22; ++u) { __builtin_amdgcn_sched_group_barrier(0x8, 1, 0); __builtin_amdgcn_sched_group_barrier(0x100, 1, 0); }
                __builtin_amdgcn_sched_group_barrier(0x8, 8, 0); }
            SBAR();
            if constexpr (s < 4) XLD(s + 4);
            if constexpr (s == 4) tag = mktag(*(LAS const unsigned long long*)(lds + EP_OFF));
            if constexpr (s == 9) { VM0(); if (lane == 0) __hip_atomic_store((gu64*)(ws + WS_KVFLAG) + ((4 * g + w) * 32 + i) * FS, tag, RLX_AGENT); }
        });
        u32x2 kf[5], vf[5], of[4];
#pragma unroll
        for (int t = 0; t < 5; ++t) { kf[t] = pack4(k[t]); vf[t] = pack4(v[t]); }
        const u32x2 one2 = {0x3F803F80u, 0x3F803F80u};
#pragma unroll
        for (int sb = 0; sb < 4; ++sb) {
            const f32x4 z = {}; const u32x2 qs = pack4(q[sb + 1]);
            const f32x4 sp = mfma16k16(kf[sb], qs, sb ? mp : mp0), sc = mfma16k16(kf[sb + 1], qs, mc);
            f32x4 s;
#pragma unroll
            for (int r = 0; r < 4; ++r) s[r] = mx(sp[r], sc[r]);
            const float m = xmax32(xmax16(mx(mx(mx(s[0], s[1]), s[2]), s[3]))) * QSCALE;
#pragma unroll
            for (int r = 0; r < 4; ++r) s[r] = __builtin_amdgcn_exp2f(__builtin_fmaf(s[r], QSCALE, -m));
            const u32x2 pk = pack4(s);
            const f32x4 l = mfma16k16(one2, pk, z);
            f32x4 o = mfma16k16(vf[sb], pk & ~bc, z); o = mfma16k16(vf[sb + 1], pk & bc, o);
            o *= __builtin_amdgcn_rcpf(l[0]);
            of[sb] = pack4(o);
        }
        u32x4 oa0 = join16(of[0], of[1]), oa1 = join16(of[2], of[3]); PIN(oa0); PIN(oa1);
        BAR();
        __builtin_amdgcn_s_setprio(3);
        STC(oa0, WS_OA + (2 * i * 32 + 4 * g + w) * 1024);
        STC(oa1, WS_OA + ((2 * i + 1) * 32 + 4 * g + w) * 1024);
        KVLD(2); KVLD(3);
        touch((w < 2 ? woa : wob) + g3 * 128, (128 * (idx & 3) + 64 * (w & 1) + lane) * 1024, lds0 + DROP_OFF);
    }
    const int tt = w & 1, kq = w >> 1, kh = w >> 2;
    const u32x4 qa = lds128(L + SPARE_OFF + wq * 2048 + (1 - kh) * 1024 + lane16), qb = lds128(L + SPARE_OFF + wq * 2048 + kh * 1024 + lane16);
    const unsigned osrc = ((2 * i3 + tt) * 32 + 8 * kq) * 1024;
    const unsigned ovoff = (lane >> 3) * 1024 + g3 * 128 + (((lane & 7) ^ (((lane >> 4) & 1) << 2)) * 16);
    u32x4 of[8];
    unsigned long long gg, gq, fb;
    gu64* obf = (gu64*)(ws + WS_OBFLAG) + ((2 * i3 + tt) * 32 + 8 * kq) * FS;
#define POLLB() asm volatile("s_nop 4\n\tglobal_load_dwordx2 %0, %1, %2 sc1" : "=v"(fb) : "v"(fvo), "s"(obf) : "memory")
    float gv;
    const unsigned wo0_ = lds0 + kq * 16384 + (8 * (lane >> 5) + ((lane >> 2) & 3)) * 128 + ((((lane >> 3) ^ kh) & 1) * 64) + ((lg ^ kq) & 1) * 32 + (lane & 3) * 8;
    const unsigned wo1_ = lds0 + kq * 16384 + (8 * (lane >> 5) + ((lane >> 2) & 3)) * 128 + (((1 ^ (lane >> 3) ^ kh) & 1) * 64) + ((lg ^ kq) & 1) * 32 + (lane & 3) * 8;
    lds_cptr wo0, wo1; unsigned fvo, svo;
    f32x16 a0, a1;
    auto proj_a = [&]() {
        a0 = a1 = (f32x16){};
#pragma unroll
        for (int u = 0; u < 8; ++u) { a0 = mfma32(tr128(wo0 + u * 2048), of[u], a0); a1 = mfma32(tr128(wo1 + u * 2048), of[u], a1); }
        a0 *= gv; a1 *= gv; PIN(a0); PIN(a1); };
    const int sidx = (int)0xDDDDDDDDu;
    const bool real = (lane & 16) == 0;
    auto finish = [&](f32x16 o, const u32x4 qf) {
        float chk = o[8] * 0x1p15f;
#pragma unroll
        for (int r = 0; r < 8; ++r) chk = mx(chk, fabsf(o[r]));
        if (__builtin_expect(__any(!(chk < 0x1p125f && o[8] > 0x1p-60f)), 0)) {
            const f32x16 zero = {}; float m = -3e38f;
            const unsigned ksrc = WS_KV + h * 131072;
#pragma unroll 1
            for (int t = 0; t < 64; ++t) { f32x16 s = mfma32(LDC(ksrc + (t >> 2) * 8192 + (t & 3) * 1024), qf, zero);
#pragma unroll
                for (int r = 0; r < 16; ++r) m = fmaxf(m, s[r]); }
            m = xmax32(m); o = zero;
            const unsigned vl = (lane >> 5) * 512 + l15 * 16;
            const u32x4 ones = {0x3F803F80u, 0x3F803F80u, 0x3F803F80u, 0x3F803F80u};
#pragma unroll 1
            for (int t = 0; t < 64; ++t) { const unsigned ts = ksrc + (t >> 2) * 8192 + (t & 3) * 1024; f32x16 s = mfma32(LDC(ts), qf, zero);
                const u32x4 a = __builtin_amdgcn_raw_buffer_load_b128(wsr, vl, ts + 4096, 16), c = __builtin_amdgcn_raw_buffer_load_b128(wsr, vl + 256, ts + 4096, 16);
#pragma unroll
                for (int r = 0; r < 16; ++r) s[r] = __builtin_amdgcn_exp2f(s[r] - m);
                o = smf(real ? a : ones, lo8(s), o, sidx); o = smf(real ? c : ones, hi8(s), o, sidx); }
        }
        const float rl = __builtin_amdgcn_rcpf(o[8]) * (1.f - __uint_as_float((unsigned)gq));
        __builtin_amdgcn_raw_buffer_store_b128(join32(cvtpk(o[0] * rl, o[1] * rl), cvtpk(o[2] * rl, o[3] * rl), cvtpk(o[4] * rl, o[5] * rl), cvtpk(o[6] * rl, o[7] * rl)), wsr, svo, 0, 16);
    };
    {
        unsigned kba = lds0 + kh * 8192 + lane16, vba = lds0 + (real ? kh * 8192 + 4096 : ONES_OFF) + (lane >> 5) * 512 + l15 * 16;
        const int vstep = real ? 16384 : 0;
        f32x16 sa, sb, ca, cb, oa, ob;
        const u32x2 nm = *(LAS const u32x2*)(lds + SPARE_OFF + 8192 + wq * 512 + lane * 8);
        u32x4 kf = lds128((lds_cptr)(uintptr_t)kba), va = lds128((lds_cptr)(uintptr_t)vba), vc = va;
        { const f32x16 zero = {}; u32x4 z4 = {}, e0 = {lane < 32 ? 0x3F80u : 0u, 0u, 0u, 0u}; PIN(z4);
          ob = mfma32(z4, e0, zero); sb = mfma32(e0, z4, zero);
          ca = mfma32(e0, (u32x4){kh ? nm[0] : nm[1], 0u, 0u, 0u}, zero);
          cb = mfma32(e0, (u32x4){kh ? nm[1] : nm[0], 0u, 0u, 0u}, zero);
          oa = mfma32(z4, z4, zero); }
        if (w >= 4) SETW(w, 2);
        sa = mfma32(kf, qa, ca);
        auto run = [&](int n) {
            asm volatile("s_waitcnt lgkmcnt(0)\n\ts_nop 11\n"
            "1:\n\t"
            "v_exp_f32 v32, v32\n\tv_exp_f32 v33, v33\n\tv_smfmac_f32_32x32x32_bf16 %[ob], %[vc], v[56:63], %[ix]\n\tds_read_b128 %[vc], %[vb] offset:256\n\tv_exp_f32 v34, v34\n\tv_exp_f32 v35, v35\n\tv_exp_f32 v36, v36\n\tv_exp_f32 v37, v37\n\tv_mfma_f32_32x32x16_bf16 v[48:63], %[kf], %[qb], %[cb]\n\tds_read_b128 %[kf], %[kb] offset:1024\n\tv_exp_f32 v38, v38\n\tv_exp_f32 v39, v39\n\tv_exp_f32 v40, v40\n\tv_exp_f32 v41, v41\n\ts_waitcnt lgkmcnt(2)\n\tv_smfmac_f32_32x32x32_bf16 %[oa], %[va], v[32:39], %[ix]\n\tv_exp_f32 v42, v42\n\tv_exp_f32 v43, v43\n\tv_exp_f32 v44, v44\n\tv_exp_f32 v45, v45\n\tv_exp_f32 v46, v46\n\tv_exp_f32 v47, v47\n\t"
            "v_exp_f32 v48, v48\n\tv_exp_f32 v49, v49\n\ts_waitcnt lgkmcnt(0)\n\tv_smfmac_f32_32x32x32_bf16 %[oa], %[vc], v[40:47], %[ix]\n\tv_exp_f32 v50, v50\n\tv_exp_f32 v51, v51\n\tv_exp_f32 v52, v52\n\tv_exp_f32 v53, v53\n\tv_mfma_f32_32x32x16_bf16 v[32:47], %[kf], %[qa], %[ca]\n\tv_exp_f32 v54, v54\n\tv_exp_f32 v55, v55\n\tv_exp_f32 v56, v56\n\tv_exp_f32 v57, v57\n\tv_smfmac_f32_32x32x32_bf16 %[ob], %[va], v[48:55], %[ix]\n\tds_read_b128 %[va], %[vb] offset:1024\n\tv_exp_f32 v58, v58\n\tv_exp_f32 v59, v59\n\tv_exp_f32 v60, v60\n\tv_exp_f32 v61, v61\n\tv_exp_f32 v62, v62\n\tv_exp_f32 v63, v63\n\t"
            "v_exp_f32 v32, v32\n\tv_exp_f32 v33, v33\n\tv_smfmac_f32_32x32x32_bf16 %[ob], %[vc], v[56:63], %[ix]\n\tds_read_b128 %[vc], %[vb] offset:1280\n\tv_exp_f32 v34, v34\n\tv_exp_f32 v35, v35\n\tv_exp_f32 v36, v36\n\tv_exp_f32 v37, v37\n\tv_mfma_f32_32x32x16_bf16 v[48:63], %[kf], %[qb], %[cb]\n\tds_read_b128 %[kf], %[kb] offset:2048\n\tv_exp_f32 v38, v38\n\tv_exp_f32 v39, v39\n\tv_exp_f32 v40, v40\n\tv_exp_f32 v41, v41\n\ts_waitcnt lgkmcnt(2)\n\tv_smfmac_f32_32x32x32_bf16 %[oa], %[va], v[32:39], %[ix]\n\tv_exp_f32 v42, v42\n\tv_exp_f32 v43, v43\n\tv_exp_f32 v44, v44\n\tv_exp_f32 v45, v45\n\tv_exp_f32 v46, v46\n\tv_exp_f32 v47, v47\n\t"
            "v_exp_f32 v48, v48\n\tv_exp_f32 v49, v49\n\ts_waitcnt lgkmcnt(0)\n\tv_smfmac_f32_32x32x32_bf16 %[oa], %[vc], v[40:47], %[ix]\n\tv_exp_f32 v50, v50\n\tv_exp_f32 v51, v51\n\tv_exp_f32 v52, v52\n\tv_exp_f32 v53, v53\n\tv_mfma_f32_32x32x16_bf16 v[32:47], %[kf], %[qa], %[ca]\n\tv_exp_f32 v54, v54\n\tv_exp_f32 v55, v55\n\tv_exp_f32 v56, v56\n\tv_exp_f32 v57, v57\n\tv_smfmac_f32_32x32x32_bf16 %[ob], %[va], v[48:55], %[ix]\n\tds_read_b128 %[va], %[vb] offset:2048\n\tv_exp_f32 v58, v58\n\tv_exp_f32 v59, v59\n\tv_exp_f32 v60, v60\n\tv_exp_f32 v61, v61\n\tv_exp_f32 v62, v62\n\tv_exp_f32 v63, v63\n\t"
            "v_exp_f32 v32, v32\n\tv_exp_f32 v33, v33\n\tv_smfmac_f32_32x32x32_bf16 %[ob], %[vc], v[56:63], %[ix]\n\tds_read_b128 %[vc], %[vb] offset:2304\n\tv_exp_f32 v34, v34\n\tv_exp_f32 v35, v35\n\tv_exp_f32 v36, v36\n\tv_exp_f32 v37, v37\n\tv_mfma_f32_32x32x16_bf16 v[48:63], %[kf], %[qb], %[cb]\n\tds_read_b128 %[kf], %[kb] offset:3072\n\tv_exp_f32 v38, v38\n\tv_exp_f32 v39, v39\n\tv_exp_f32 v40, v40\n\tv_exp_f32 v41, v41\n\ts_waitcnt lgkmcnt(2)\n\tv_smfmac_f32_32x32x32_bf16 %[oa], %[va], v[32:39], %[ix]\n\tv_exp_f32 v42, v42\n\tv_exp_f32 v43, v43\n\tv_exp_f32 v44, v44\n\tv_exp_f32 v45, v45\n\tv_exp_f32 v46, v46\n\tv_exp_f32 v47, v47\n\t"
            "v_exp_f32 v48, v48\n\tv_exp_f32 v49, v49\n\ts_waitcnt lgkmcnt(0)\n\tv_smfmac_f32_32x32x32_bf16 %[oa], %[vc], v[40:47], %[ix]\n\tv_exp_f32 v50, v50\n\tv_exp_f32 v51, v51\n\tv_exp_f32 v52, v52\n\tv_exp_f32 v53, v53\n\tv_mfma_f32_32x32x16_bf16 v[32:47], %[kf], %[qa], %[ca]\n\tv_exp_f32 v54, v54\n\tv_exp_f32 v55, v55\n\tv_exp_f32 v56, v56\n\tv_exp_f32 v57, v57\n\tv_smfmac_f32_32x32x32_bf16 %[ob], %[va], v[48:55], %[ix]\n\tds_read_b128 %[va], %[vb] offset:3072\n\tv_exp_f32 v58, v58\n\tv_exp_f32 v59, v59\n\tv_exp_f32 v60, v60\n\tv_exp_f32 v61, v61\n\tv_exp_f32 v62, v62\n\tv_exp_f32 v63, v63\n\t"
            "v_exp_f32 v32, v32\n\tv_exp_f32 v33, v33\n\tv_smfmac_f32_32x32x32_bf16 %[ob], %[vc], v[56:63], %[ix]\n\tds_read_b128 %[vc], %[vb] offset:3328\n\tv_exp_f32 v34, v34\n\tv_exp_f32 v35, v35\n\tv_exp_f32 v36, v36\n\tv_exp_f32 v37, v37\n\tv_mfma_f32_32x32x16_bf16 v[48:63], %[kf], %[qb], %[cb]\n\tds_read_b128 %[kf], %[kb] offset:16384\n\tv_exp_f32 v38, v38\n\tv_exp_f32 v39, v39\n\tv_exp_f32 v40, v40\n\tv_exp_f32 v41, v41\n\ts_waitcnt lgkmcnt(2)\n\tv_smfmac_f32_32x32x32_bf16 %[oa], %[va], v[32:39], %[ix]\n\tv_exp_f32 v42, v42\n\tv_exp_f32 v43, v43\n\tv_exp_f32 v44, v44\n\tv_exp_f32 v45, v45\n\tv_exp_f32 v46, v46\n\tv_exp_f32 v47, v47\n\t"
            "v_exp_f32 v48, v48\n\tv_exp_f32 v49, v49\n\ts_waitcnt lgkmcnt(0)\n\tv_smfmac_f32_32x32x32_bf16 %[oa], %[vc], v[40:47], %[ix]\n\tv_exp_f32 v50, v50\n\tv_exp_f32 v51, v51\n\tv_exp_f32 v52, v52\n\tv_exp_f32 v53, v53\n\tv_mfma_f32_32x32x16_bf16 v[32:47], %[kf], %[qa], %[ca]\n\tv_exp_f32 v54, v54\n\tv_exp_f32 v55, v55\n\tv_exp_f32 v56, v56\n\tv_exp_f32 v57, v57\n\tv_add_u32 %[vb], %[vb], %[vs]\n\tv_smfmac_f32_32x32x32_bf16 %[ob], %[va], v[48:55], %[ix]\n\tds_read_b128 %[va], %[vb] offset:0\n\tv_add_u32 %[kb], 0x4000, %[kb]\n\ts_sub_u32 %[n], %[n], 1\n\tv_exp_f32 v58, v58\n\tv_exp_f32 v59, v59\n\tv_exp_f32 v60, v60\n\tv_exp_f32 v61, v61\n\tv_exp_f32 v62, v62\n\tv_exp_f32 v63, v63\n\t"
            "s_cmp_lg_u32 %[n], 0\n\ts_cbranch_scc1 1b\n\ts_waitcnt lgkmcnt(0)\n\ts_nop 7"
            : "+{v[32:47]}"(sa), "+{v[48:63]}"(sb), [oa] "+v"(oa), [ob] "+v"(ob), [kf] "+v"(kf), [va] "+v"(va), [vc] "+v"(vc), [kb] "+v"(kba), [vb] "+v"(vba), [n] "+s"(n)
            : [qa] "v"(qa), [qb] "v"(qb), [ca] "v"(ca), [cb] "v"(cb), [vs] "v"(vstep), [ix] "v"(sidx) : "scc", "memory");
        };
        run(2);
        if (w < 4) { VM0(); if (lane == 0) __hip_atomic_store((gu64*)(ws + WS_OAFLAG) + (i * 32 + 4 * g + w) * FS, tag, RLX_AGENT); SETW(w, 2); }
        WAITM(0, 7, 2);
        __builtin_amdgcn_s_setprio(2);
        run(2);
        SETW(8 + w, 1);
        WAITW(8 + 4 * kh, 1);
#pragma unroll
        for (int u = 0; u < 8; ++u) glds1(woa + (size_t)(64 * (2 * wq + kh) + 8 * u) * 1024, ovoff, lds0 + (2 * wq + kh) * 8192 + u * 1024);
        unsigned long long fa;
        const unsigned go = WS_GATE + (64 * i3 + 32 * tt + (lane & 31)) * 8, qo = WS_GATE + (256 * j + 64 * wq + 32 * kh + (lane & 31)) * 8;
        asm volatile("s_nop 4\n\tglobal_load_dwordx2 %0, %3, %6 sc1\n\tglobal_load_dwordx2 %1, %4, %6 sc1\n\tglobal_load_dwordx2 %2, %5, %6 sc1" : "=&v"(fa), "=&v"(gg), "=&v"(gq)
                     : "v"(WS_OAFLAG + (i3 * 32 + 8 * kq + (lane & 7)) * FS * 8), "v"(go), "v"(qo), "s"(ws) : "memory");
        __builtin_amdgcn_s_setprio(1);
        run(1);
        asm volatile("s_waitcnt vmcnt(0)" : "+v"(fa), "+v"(gg), "+v"(gq) :: "memory");
#pragma unroll 1
        for (unsigned spins = 0; spins < kSpinCap && !__all(fa == tag && (gg >> 32) == (tag >> 32) && (gq >> 32) == (tag >> 32)); ++spins) {
            fa = __hip_atomic_load((gu64*)(ws + WS_OAFLAG) + (i3 * 32 + 8 * kq + (lane & 7)) * FS, RLX_AGENT);
            gg = __hip_atomic_load((gu64*)(ws + go), RLX_AGENT); gq = __hip_atomic_load((gu64*)(ws + qo), RLX_AGENT); }
        __builtin_amdgcn_fence(__ATOMIC_ACQUIRE, "wavefront");
#pragma unroll
        for (int u = 0; u < 8; ++u) of[u] = LDC(WS_OA + osrc + u * 1024);
        SETW(8 + w, 2);
        unsigned xw = lds0 + SPARE_OFF + w * 3072 + lane16, xr = lds0 + SPARE_OFF + (w ^ 4) * 3072 + lane16; PIN(xw); PIN(xr);
        { unsigned p0 = wo0_, p1 = wo1_; fvo = (lane & 7) * 8 * FS; svo = WS_OB + ((8 * j + 2 * wq + kh) * 32 + h) * 1024 + lane16;
          PIN(p0); PIN(p1); PIN(fvo); PIN(svo); wo0 = (lds_cptr)(uintptr_t)p0; wo1 = (lds_cptr)(uintptr_t)p1; }
        __builtin_amdgcn_s_setprio(0);
        run(3);
        *(LAS f32x4*)(uintptr_t)xw = __builtin_shufflevector(oa, oa, 0, 1, 2, 3); *(LAS f32x4*)(uintptr_t)(xw + 1024) = __builtin_shufflevector(oa, oa, 4, 5, 6, 7); *(LAS float*)(uintptr_t)(xw + 2048) = oa[8];
        SBAR();
        f32x16 o = smf(vc, hi8(sb), ob, sidx);
        asm volatile("" : "+v"(of[0]), "+v"(of[1]), "+v"(of[2]), "+v"(of[3]), "+v"(of[4]), "+v"(of[5]), "+v"(of[6]), "+v"(of[7]));
        if (w >= 4) POLLB();
        SBAR();
        gv = __uint_as_float((unsigned)gg);
        if (w < 4) {
            WAITW(8, 2); WAITW(12, 2);
            proj_a();
            SETW(8 + w, 3); WAITW(8, 3);
#pragma unroll
            for (int u = 0; u < 16; ++u) glds1(wob + (size_t)(128 * w + 8 * u) * 1024, ovoff, lds0 + (w < 2 ? w * 16384 + u * 1024 : w * 32768 + (u >> 3) * 16384 + (u & 7) * 1024));
            VM0(); POLLB(); }
        BAR();
        const f32x4 r0 = *(LAS const f32x4*)(uintptr_t)xr, r1 = *(LAS const f32x4*)(uintptr_t)(xr + 1024);
#pragma unroll
        for (int r = 0; r < 4; ++r) { o[r] += r0[r]; o[4 + r] += r1[r]; }
        o[8] += *(LAS const float*)(uintptr_t)(xr + 2048);
        finish(o, qb);
    }
    {
        u32x4 wf[16], ob8[8];
        asm volatile("s_waitcnt vmcnt(0)" : "+v"(fb) :: "memory");
        if (lane == 0) __hip_atomic_store((gu64*)(ws + WS_OBFLAG) + ((8 * j + 2 * wq + kh) * 32 + h) * FS, tag, RLX_AGENT);
        unsigned got = (unsigned)__builtin_amdgcn_ballot_w64(fb == tag) & 0xffu;
        const char* pb = ws + WS_OB + osrc + 4096;
        asm volatile("s_nop 4\n\t"
            "s_bitcmp1_b32 %[got], 0\n\ts_cbranch_scc0 10f\n\tglobal_load_dwordx4 %0, %[vo], %[pb] offset:-4096 sc1\n10:\n\t"
            "s_bitcmp1_b32 %[got], 1\n\ts_cbranch_scc0 11f\n\tglobal_load_dwordx4 %1, %[vo], %[pb] offset:-3072 sc1\n11:\n\t"
            "s_bitcmp1_b32 %[got], 2\n\ts_cbranch_scc0 12f\n\tglobal_load_dwordx4 %2, %[vo], %[pb] offset:-2048 sc1\n12:\n\t"
            "s_bitcmp1_b32 %[got], 3\n\ts_cbranch_scc0 13f\n\tglobal_load_dwordx4 %3, %[vo], %[pb] offset:-1024 sc1\n13:\n\t"
            "s_bitcmp1_b32 %[got], 4\n\ts_cbranch_scc0 14f\n\tglobal_load_dwordx4 %4, %[vo], %[pb] offset:0 sc1\n14:\n\t"
            "s_bitcmp1_b32 %[got], 5\n\ts_cbranch_scc0 15f\n\tglobal_load_dwordx4 %5, %[vo], %[pb] offset:1024 sc1\n15:\n\t"
            "s_bitcmp1_b32 %[got], 6\n\ts_cbranch_scc0 16f\n\tglobal_load_dwordx4 %6, %[vo], %[pb] offset:2048 sc1\n16:\n\t"
            "s_bitcmp1_b32 %[got], 7\n\ts_cbranch_scc0 17f\n\tglobal_load_dwordx4 %7, %[vo], %[pb] offset:3072 sc1\n17:\n\t"
            : "=&v"(ob8[0]), "=&v"(ob8[1]), "=&v"(ob8[2]), "=&v"(ob8[3]), "=&v"(ob8[4]), "=&v"(ob8[5]), "=&v"(ob8[6]), "=&v"(ob8[7])
            : [got] "s"(got), [pb] "s"(pb), [vo] "v"(lane16) : "scc", "memory");
        if (w >= 4) { proj_a(); SETW(8 + w, 3); }
        const int wb0 = kq < 2 ? 0 : kq * 16384, wb1 = kq < 2 ? 0 : kq * 16384 + 8192;
#pragma unroll
        for (int u = 0; u < 8; ++u) { wf[2 * u] = tr128(wo0 + (u < 4 ? wb0 : wb1) + u * 2048); wf[2 * u + 1] = tr128(wo1 + (u < 4 ? wb0 : wb1) + u * 2048); }
        WAITW(12, 3);
        const unsigned rb = lds0 + (tt ? 73728 : 32768) + lane16, cs = tt ? 16384 : 8192;
        unsigned wa1 = rb + (kq ^ 1) * cs + 2048, wa2 = rb + (kq ^ 2) * cs + 4096, wa3 = rb + (kq ^ 3) * cs + 6144, ra = rb + kq * cs;
        unsigned oo = (64 * i3 + 32 * tt + (lane & 31)) * 1024 + g3 * 128 + kq * 32 + (lane >> 5) * 16;
        PIN(wa1); PIN(wa2); PIN(wa3); PIN(ra); PIN(oo);
        { unsigned fresh, spins = kSpinCap;
          asm volatile("s_nop 4\n\t"
            "s_cmp_eq_u32 %[got], 0xff\n\ts_cbranch_scc1 3f\n2:\n\t"
            "global_load_dwordx2 %[fb], %[fo], %[fp] sc1\n\ts_waitcnt vmcnt(0)\n\t"
            "v_cmp_eq_u64 vcc, %[tg], %[fb]\n\ts_nop 1\n\ts_and_b32 %[fr], vcc_lo, 0xff\n\ts_andn2_b32 %[fr], %[fr], %[got]\n\t"
            "s_bitcmp1_b32 %[fr], 0\n\ts_cbranch_scc0 10f\n\tglobal_load_dwordx4 %0, %[vo], %[pb] offset:-4096 sc1\n10:\n\t"
            "s_bitcmp1_b32 %[fr], 1\n\ts_cbranch_scc0 11f\n\tglobal_load_dwordx4 %1, %[vo], %[pb] offset:-3072 sc1\n11:\n\t"
            "s_bitcmp1_b32 %[fr], 2\n\ts_cbranch_scc0 12f\n\tglobal_load_dwordx4 %2, %[vo], %[pb] offset:-2048 sc1\n12:\n\t"
            "s_bitcmp1_b32 %[fr], 3\n\ts_cbranch_scc0 13f\n\tglobal_load_dwordx4 %3, %[vo], %[pb] offset:-1024 sc1\n13:\n\t"
            "s_bitcmp1_b32 %[fr], 4\n\ts_cbranch_scc0 14f\n\tglobal_load_dwordx4 %4, %[vo], %[pb] offset:0 sc1\n14:\n\t"
            "s_bitcmp1_b32 %[fr], 5\n\ts_cbranch_scc0 15f\n\tglobal_load_dwordx4 %5, %[vo], %[pb] offset:1024 sc1\n15:\n\t"
            "s_bitcmp1_b32 %[fr], 6\n\ts_cbranch_scc0 16f\n\tglobal_load_dwordx4 %6, %[vo], %[pb] offset:2048 sc1\n16:\n\t"
            "s_bitcmp1_b32 %[fr], 7\n\ts_cbranch_scc0 17f\n\tglobal_load_dwordx4 %7, %[vo], %[pb] offset:3072 sc1\n17:\n\t"
            "s_or_b32 %[got], %[got], %[fr]\n\ts_cmp_eq_u32 %[got], 0xff\n\ts_cbranch_scc1 3f\n\ts_sleep 1\n\ts_sub_u32 %[sp], %[sp], 1\n\ts_cbranch_scc0 2b\n"
            "3:\n\ts_waitcnt vmcnt(0)"
            : "+v"(ob8[0]), "+v"(ob8[1]), "+v"(ob8[2]), "+v"(ob8[3]), "+v"(ob8[4]), "+v"(ob8[5]), "+v"(ob8[6]), "+v"(ob8[7]), [got] "+s"(got), [fr] "=&s"(fresh), [sp] "+s"(spins), [fb] "=&v"(fb)
            : [pb] "s"(pb), [vo] "v"(lane16), [fo] "v"(fvo), [fp] "s"(obf), [tg] "s"(tag) : "scc", "vcc", "memory"); }
#pragma unroll
        for (int u = 0; u < 8; ++u) { a1 = mfma32(wf[2 * u + 1], ob8[u], a1); a0 = mfma32(wf[2 * u], ob8[u], a0); }
        *(LAS f32x4*)(uintptr_t)wa2 = __builtin_shufflevector(a1, a1, 0, 1, 2, 3); *(LAS f32x4*)(uintptr_t)(wa2 + 1024) = __builtin_shufflevector(a1, a1, 4, 5, 6, 7);
        *(LAS f32x4*)(uintptr_t)wa3 = __builtin_shufflevector(a1, a1, 8, 9, 10, 11); *(LAS f32x4*)(uintptr_t)(wa3 + 1024) = __builtin_shufflevector(a1, a1, 12, 13, 14, 15);
        *(LAS f32x4*)(uintptr_t)wa1 = __builtin_shufflevector(a0, a0, 8, 9, 10, 11); *(LAS f32x4*)(uintptr_t)(wa1 + 1024) = __builtin_shufflevector(a0, a0, 12, 13, 14, 15);
        BAR();
        LAS const f32x4* rr = (LAS const f32x4*)(uintptr_t)ra;
        const f32x4 r0 = (__builtin_shufflevector(a0, a0, 0, 1, 2, 3) + rr[128]) + (rr[256] + rr[384]), r1 = (__builtin_shufflevector(a0, a0, 4, 5, 6, 7) + rr[192]) + (rr[320] + rr[448]);
        *(u32x4*)(out + oo) =
            join32(cvtpk(r0[0], r0[1]), cvtpk(r0[2], r0[3]), cvtpk(r1[0], r1[1]), cvtpk(r1[2], r1[3]));
        if (blockIdx.x == 0 && tid == 0) __hip_atomic_store((gu64*)(ws + WS_EPOCH), tag, RLX_AGENT);
    }
}
}

extern "C" void kernel_launch(void* const* d_in, const int* in_sizes, int n_in, void* d_out, int out_size, void* d_ws, size_t ws_size, hipStream_t stream) {
    fused_kernel<<<256, 512, 0, stream>>>((const char*)d_in[0], (const char*)d_in[1], (const char*)d_in[2], (const char*)d_in[4], (const char*)d_in[5],
                                          (const char*)d_in[6], (char*)d_out, (char*)d_ws);
}
```
